```python
import torch

def _softmax(x, axis=-1):
    return torch.softmax(x, dim=axis)

def _gelu(x):
    return torch.nn.functional.gelu(x, approximate='tanh')

def _silu(x):
    return torch.nn.functional.silu(x)

def _sigmoid(x):
    return torch.sigmoid(x)

def _layernorm(x, axis=-1, eps=1e-05):
    m = x.mean(dim=axis, keepdim=True)
    v = x.var(dim=axis, keepdim=True, unbiased=False)
    return (x - m) / torch.sqrt(v + eps)

def _scatter_add(tgt, idx, src, axis=0):
    return tgt.clone().index_add_(axis, idx, src)

def _segment_sum(src, seg, n):
    out = torch.zeros((n,) + src.shape[1:], dtype=src.dtype)
    return out.index_add_(0, seg, src)

def _pad(x, pw):
    flat = tuple((v for pair in reversed(pw) for v in pair))
    return torch.nn.functional.pad(x, flat)
kernel_name = 'standalone__tree_speculative_verify_tree_attention_over_candidate_token_tree__decode'

def setup_inputs():

    _w = lambda a: (lambda t: t.to(torch.bfloat16) if t.is_floating_point() else t)(torch.as_tensor(a))
    B, Hq, Hkv, T, C, D = (1, 32, 16, 1, 32768, 128)
    tree_mask = torch.zeros((B, T, T), dtype=float)
    for b in range(B):
        parent = torch.zeros(T, dtype=torch.int64)
        for i in range(1, T):
            parent[i] = int(torch.randint(0, i, (1,)).item())
        for i in range(T):
            j = i
            tree_mask[b, i, j] = 1.0
            while j > 0:
                j = parent[j]
                tree_mask[b, i, j] = 1.0
    return {'q': _w((lambda *s: torch.randn(*s, dtype=torch.bfloat16) * 0.8)(B, Hq, T, D)), 'k_cache': _w((lambda *s: torch.randn(*s, dtype=torch.bfloat16) * 0.8)(B, Hkv, C, D)), 'v_cache': _w((lambda *s: torch.randn(*s, dtype=torch.bfloat16) * 0.05)(B, Hkv, C, D)), 'k_tree': _w((lambda *s: torch.randn(*s, dtype=torch.bfloat16) * 0.8)(B, Hkv, T, D)), 'v_tree': _w((lambda *s: torch.randn(*s, dtype=torch.bfloat16) * 0.05)(B, Hkv, T, D)), 'tree_mask': _w(tree_mask)}

def reference(q, k_cache, v_cache, k_tree, v_tree, tree_mask):
    B, Hq, T, D = q.shape
    Hkv, C = (k_cache.shape[1], k_cache.shape[2])
    rep = Hq // Hkv
    kc = (lambda x, n, axis: torch.repeat_interleave(x, n, dim=axis))(k_cache, rep, axis=1)
    vc = (lambda x, n, axis: torch.repeat_interleave(x, n, dim=axis))(v_cache, rep, axis=1)
    kt = (lambda x, n, axis: torch.repeat_interleave(x, n, dim=axis))(k_tree, rep, axis=1)
    vt = (lambda x, n, axis: torch.repeat_interleave(x, n, dim=axis))(v_tree, rep, axis=1)
    k = torch.concatenate([kc, kt], axis=2)
    v = torch.concatenate([vc, vt], axis=2)
    scale = D ** (-0.5)
    scores = torch.einsum('bhqd,bhkd->bhqk', q, k) * scale
    ctx_mask = torch.ones((B, 1, T, C), dtype=scores.dtype)
    tm = tree_mask[:, None, :, :].clone()
    full_mask = torch.concatenate([ctx_mask, tm], axis=-1)
    scores = scores + (1.0 - full_mask) * -1000000000.0
    p = _softmax(scores, axis=-1)
    return torch.einsum('bhqk,bhkd->bhqd', p, v)
```

```cpp
#include <hip/hip_runtime.h>
#include <hip/hip_bf16.h>
#include <stdint.h>

#define NWAVE 8
#define NSPLIT 16
#define DEPTH 3
#define USE_NT 1

namespace {
constexpr int HQ = 32, HKV = 16, CTX = 32768, D = 128, G = HQ / HKV;
constexpr int TOK_PER_WG = CTX / NSPLIT;
constexpr int TILES_PER_WAVE = TOK_PER_WG / 16 / NWAVE;
constexpr float C2 = 0.088388347648318440f * 1.4426950408889634f;

typedef unsigned u32x4 __attribute__((ext_vector_type(4)));
typedef float f32x4 __attribute__((ext_vector_type(4)));
typedef short bf16x8 __attribute__((ext_vector_type(8)));
typedef __bf16 bf16x8_t __attribute__((ext_vector_type(8)));
#define GAS __attribute__((address_space(1)))

__device__ __forceinline__ u32x4 ldg(const GAS u32x4* p) {
  return __builtin_nontemporal_load(p);
}
__device__ __forceinline__ f32x4 mfma16(u32x4 a, u32x4 b, f32x4 c) {
  return __builtin_amdgcn_mfma_f32_16x16x32_bf16(__builtin_bit_cast(bf16x8_t, a), __builtin_bit_cast(bf16x8_t, b), c, 0, 0, 0);
}
__device__ __forceinline__ unsigned cvtpk(float lo, float hi) {
  typedef __bf16 bf16x2_t __attribute__((ext_vector_type(2)));
  typedef float f32x2 __attribute__((ext_vector_type(2)));
  f32x2 v = {lo, hi};
  return __builtin_bit_cast(unsigned, __builtin_convertvector(v, bf16x2_t));
}
__device__ __forceinline__ float xrow_max(float x) {
  auto s = __builtin_amdgcn_permlane16_swap(__float_as_uint(x), __float_as_uint(x), false, false);
  x = fmaxf(__uint_as_float(s[0]), __uint_as_float(s[1]));
  auto t = __builtin_amdgcn_permlane32_swap(__float_as_uint(x), __float_as_uint(x), false, false);
  return fmaxf(__uint_as_float(t[0]), __uint_as_float(t[1]));
}
__device__ __forceinline__ float xrow_sum(float x) {
  auto s = __builtin_amdgcn_permlane16_swap(__float_as_uint(x), __float_as_uint(x), false, false);
  x = __uint_as_float(s[0]) + __uint_as_float(s[1]);
  auto t = __builtin_amdgcn_permlane32_swap(__float_as_uint(x), __float_as_uint(x), false, false);
  return __uint_as_float(t[0]) + __uint_as_float(t[1]);
}
__device__ __forceinline__ float bf2f(unsigned short b) { return __uint_as_float((unsigned)b << 16); }

constexpr int REC = 2 * D + 4;

__global__ void __launch_bounds__(NWAVE * 64) attn_split(const unsigned short* __restrict__ q, const unsigned short* __restrict__ kc,
                                                         const unsigned short* __restrict__ vc, float* __restrict__ part) {
  __shared__ __attribute__((aligned(16))) float lds[NWAVE * REC];
  const int tid = threadIdx.x, lane = tid & 63, wave = __builtin_amdgcn_readfirstlane(tid >> 6);
  const int a = lane >> 4, n = lane & 15;
  const int h = blockIdx.x / NSPLIT, s = blockIdx.x % NSPLIT;
  const size_t base = ((size_t)h * CTX + (size_t)s * TOK_PER_WG + wave * 16) * (D * 2);
  const GAS char* kp = (const GAS char*)kc + base + n * 256 + a * 16;
  const GAS char* vp = (const GAS char*)vc + base + a * 1024 + n * 16;
  constexpr int TSTR = NWAVE * 16 * 256;

  u32x4 K[DEPTH][4], V[DEPTH][4];
#pragma unroll
  for (int i = 0; i < DEPTH; ++i) {
#pragma unroll
    for (int j = 0; j < 4; ++j) K[i][j] = ldg((const GAS u32x4*)(kp + (size_t)i * TSTR + j * 64));
#pragma unroll
    for (int j = 0; j < 4; ++j) V[i][j] = ldg((const GAS u32x4*)(vp + (size_t)i * TSTR + j * 256));
  }
  u32x4 Q[4];
  {
    const GAS char* qp = (const GAS char*)q + (size_t)(h * G + (n >> 3)) * 256 + a * 16;
#pragma unroll
    for (int j = 0; j < 4; ++j) Q[j] = *(const GAS u32x4*)(qp + j * 64);
  }
  unsigned msk[4];
#pragma unroll
  for (int w = 0; w < 4; ++w) msk[w] = ((n & 7) >> 1) == w ? ((n & 1) ? 0xffff0000u : 0x0000ffffu) : 0u;

  float m = -INFINITY, l = 0.f;
  f32x4 O = {0.f, 0.f, 0.f, 0.f};
#pragma unroll
  for (int i = 0; i < TILES_PER_WAVE; ++i) {
    const int b = i % DEPTH;
    f32x4 S = {0.f, 0.f, 0.f, 0.f};
#pragma unroll
    for (int j = 0; j < 4; ++j) S = mfma16(K[b][j], Q[j], S);
    if (i + DEPTH < TILES_PER_WAVE) {
#pragma unroll
      for (int j = 0; j < 4; ++j) K[b][j] = ldg((const GAS u32x4*)(kp + (size_t)(i + DEPTH) * TSTR + j * 64));
    }
    const float tm = xrow_max(fmaxf(fmaxf(S[0], S[1]), fmaxf(S[2], S[3])));
    const float mn = fmaxf(m, tm);
    const float mc = mn * C2;
    const float alpha = __builtin_amdgcn_exp2f(m * C2 - mc);
    m = mn;
    float p[4];
#pragma unroll
    for (int j = 0; j < 4; ++j) p[j] = __builtin_amdgcn_exp2f(__builtin_fmaf(S[j], C2, -mc));
    l = l * alpha + ((p[0] + p[1]) + (p[2] + p[3]));
    O *= alpha;
#pragma unroll
    for (int j = 0; j < 4; ++j) {
      const unsigned pp = cvtpk(p[j], p[j]);
      const u32x4 B = {pp & msk[0], pp & msk[1], pp & msk[2], pp & msk[3]};
      O = mfma16(V[b][j], B, O);
    }
    if (i + DEPTH < TILES_PER_WAVE) {
#pragma unroll
      for (int j = 0; j < 4; ++j) V[b][j] = ldg((const GAS u32x4*)(vp + (size_t)(i + DEPTH) * TSTR + j * 256));
    }
  }
  l = xrow_sum(l);
  float* rec = lds + wave * REC;
#pragma unroll
  for (int j = 0; j < 4; ++j) rec[(n >> 3) * D + 32 * a + 8 * j + (n & 7)] = O[j];
  if (a == 0 && (n & 7) == 0) { rec[2 * D + 2 * (n >> 3)] = m * C2; rec[2 * D + 2 * (n >> 3) + 1] = l; }
  __syncthreads();
  if (tid < 2 * D) {
    const int g = tid >> 7;
    float M = -INFINITY;
#pragma unroll
    for (int w = 0; w < NWAVE; ++w) M = fmaxf(M, lds[w * REC + 2 * D + 2 * g]);
    float L = 0.f, o = 0.f;
#pragma unroll
    for (int w = 0; w < NWAVE; ++w) {
      const float wt = __builtin_amdgcn_exp2f(lds[w * REC + 2 * D + 2 * g] - M);
      L = __builtin_fmaf(wt, lds[w * REC + 2 * D + 2 * g + 1], L);
      o = __builtin_fmaf(wt, lds[w * REC + tid], o);
    }
    float* out = part + (size_t)blockIdx.x * REC;
    out[tid] = o;
    if ((tid & 127) == 0) { out[2 * D + 2 * g] = M; out[2 * D + 2 * g + 1] = L; }
  }
}

__global__ void __launch_bounds__(128) attn_combine(const unsigned short* __restrict__ q, const unsigned short* __restrict__ kt,
                                                    const unsigned short* __restrict__ vt, const unsigned short* __restrict__ mask,
                                                    const float* __restrict__ part, __hip_bfloat16* __restrict__ out) {
  __shared__ float red[2];
  const int hq = blockIdx.x, h = hq / G, g = hq % G, d = threadIdx.x;
  float o[NSPLIT], ms[NSPLIT], ls[NSPLIT];
#pragma unroll
  for (int s = 0; s < NSPLIT; ++s) {
    const float* rec = part + (size_t)(h * NSPLIT + s) * REC;
    o[s] = rec[g * D + d]; ms[s] = rec[2 * D + 2 * g]; ls[s] = rec[2 * D + 2 * g + 1];
  }
  float dot = bf2f(q[hq * D + d]) * bf2f(kt[h * D + d]);
  const float vtd = bf2f(vt[h * D + d]);
  const float mk = bf2f(mask[0]);
#pragma unroll
  for (int off = 32; off > 0; off >>= 1) dot += __shfl_xor(dot, off);
  if ((d & 63) == 0) red[d >> 6] = dot;
  __syncthreads();
  const float st = ((red[0] + red[1]) * 0.088388347648318440f + (1.0f - mk) * -1000000000.0f) * 1.4426950408889634f;
  float M = st;
#pragma unroll
  for (int s = 0; s < NSPLIT; ++s) M = fmaxf(M, ms[s]);
  const float wt0 = __builtin_amdgcn_exp2f(st - M);
  float L = wt0, acc = wt0 * vtd;
#pragma unroll
  for (int s = 0; s < NSPLIT; ++s) {
    const float wt = __builtin_amdgcn_exp2f(ms[s] - M);
    L = __builtin_fmaf(wt, ls[s], L);
    acc = __builtin_fmaf(wt, o[s], acc);
  }
  out[hq * D + d] = __float2bfloat16(acc / L);
}
}

extern "C" void kernel_launch(void* const* d_in, const int* in_sizes, int n_in, void* d_out, int out_size, void* d_ws, size_t ws_size,
                              hipStream_t stream) {
  const unsigned short* q = (const unsigned short*)d_in[0];
  const unsigned short* kc = (const unsigned short*)d_in[1];
  const unsigned short* vc = (const unsigned short*)d_in[2];
  const unsigned short* kt = (const unsigned short*)d_in[3];
  const unsigned short* vt = (const unsigned short*)d_in[4];
  const unsigned short* mask = (const unsigned short*)d_in[5];
  float* part = (float*)d_ws;
  attn_split<<<HKV * NSPLIT, NWAVE * 64, 0, stream>>>(q, kc, vc, part);
  attn_combine<<<HQ, 128, 0, stream>>>(q, kt, vt, mask, part, (__hip_bfloat16*)d_out);
}
```

```cpp
#include <hip/hip_runtime.h>
#include <hip/hip_bf16.h>
#include <stdint.h>

namespace {
constexpr int HQ = 32, HKV = 16, CTX = 32768, D = 128, G = HQ / HKV;
constexpr int NSPLIT = 16, NSW = 4;
constexpr int NTHR = 64 * (NSW + 1);
constexpr int TILES = CTX / 16 / NSPLIT / NSW;
constexpr int GS = 4;
constexpr unsigned KV_BYTES = (unsigned)HKV * CTX * D * 2;
constexpr unsigned TSTEP = GS * NSW * 4096;
constexpr float SCALE = 0.088388347648318440f, LOG2E = 1.4426950408889634f, C2 = SCALE * LOG2E;
constexpr unsigned PACE = 866, T0CAP = 200 * 16;
constexpr unsigned WS_X = 0, WS_Z = 2048, WS_SLOT = 4096, SLOT_BYTES = 1024 + 128, WS_HEAD = 32768;
constexpr unsigned EMPTY = 0xffffffffu;
constexpr unsigned OOB = 0x40000000u;
constexpr unsigned SPIN_CAP = 1u << 18;
constexpr int CODE_BYTES = 6144;
constexpr int L_O = 0, L_ML = NSW * 256, L_VT = L_ML + NSW * 128, L_S = L_VT + 64, L_WORDS = L_S + NSW * TILES * 32;

typedef unsigned u32x2 __attribute__((ext_vector_type(2)));
typedef unsigned u32x4 __attribute__((ext_vector_type(4)));
typedef float f32x2 __attribute__((ext_vector_type(2)));
typedef float f32x4 __attribute__((ext_vector_type(4)));
typedef __bf16 bf16x2_t __attribute__((ext_vector_type(2)));
typedef __bf16 bf16x8_t __attribute__((ext_vector_type(8)));
#define GAS __attribute__((address_space(1)))
#define RLX_AGENT __ATOMIC_RELAXED, __HIP_MEMORY_SCOPE_AGENT
#define SB() __builtin_amdgcn_sched_barrier(0)
constexpr int AUX_NT = 2, AUX_SC1 = 16, RSRC_W3 = 0x00020000;

__device__ __forceinline__ __amdgpu_buffer_rsrc_t rsrc(const void* p, unsigned bytes) { return __builtin_amdgcn_make_buffer_rsrc((void*)p, 0, bytes, RSRC_W3); }
__device__ __forceinline__ f32x4 mfma16(u32x4 a, u32x4 b, f32x4 c) {
  return __builtin_amdgcn_mfma_f32_16x16x32_bf16(__builtin_bit_cast(bf16x8_t, a), __builtin_bit_cast(bf16x8_t, b), c, 0, 0, 0);
}
__device__ __forceinline__ unsigned cvtpk(float lo, float hi) {
  f32x2 v = {lo, hi};
  return __builtin_bit_cast(unsigned, __builtin_convertvector(v, bf16x2_t));
}
__device__ __forceinline__ float vmax(float a, float b) { return __builtin_elementwise_maximum(a, b); }
__device__ __forceinline__ float xrow_max(float x) {
  auto s = __builtin_amdgcn_permlane16_swap(__float_as_uint(x), __float_as_uint(x), false, false);
  x = vmax(__uint_as_float(s[0]), __uint_as_float(s[1]));
  auto t = __builtin_amdgcn_permlane32_swap(__float_as_uint(x), __float_as_uint(x), false, false);
  return vmax(__uint_as_float(t[0]), __uint_as_float(t[1]));
}
__device__ __forceinline__ float xrow16_sum(float x) {
  auto s = __builtin_amdgcn_permlane16_swap(__float_as_uint(x), __float_as_uint(x), false, false);
  return __uint_as_float(s[0]) + __uint_as_float(s[1]);
}
__device__ __forceinline__ float xrow_sum(float x) {
  x = xrow16_sum(x);
  auto t = __builtin_amdgcn_permlane32_swap(__float_as_uint(x), __float_as_uint(x), false, false);
  return __uint_as_float(t[0]) + __uint_as_float(t[1]);
}
template <int CTRL> __device__ __forceinline__ float dpp(float x) {
  return __builtin_bit_cast(float, __builtin_amdgcn_mov_dpp(__builtin_bit_cast(int, x), CTRL, 0xf, 0xf, true));
}
constexpr int ROR1 = 0x121, ROR2 = 0x122, ROR4 = 0x124, ROR8 = 0x128, QUAD_EVEN = 0xa0  , QUAD_ODD = 0xf5  ;
__device__ __forceinline__ float row_sum(float x) { x += dpp<ROR8>(x); x += dpp<ROR4>(x); x += dpp<ROR2>(x); return x + dpp<ROR1>(x); }
__device__ __forceinline__ float bflo(unsigned u) { return __uint_as_float(u << 16); }
__device__ __forceinline__ float bfhi(unsigned u) { return __uint_as_float(u & 0xffff0000u); }
__device__ __forceinline__ unsigned not_empty(float x) { return min(__float_as_uint(x), EMPTY - 1u); }
template <int N> __device__ __forceinline__ void waitvm() { asm volatile("s_waitcnt vmcnt(%0)" ::"n"(N) : "memory"); }
__device__ __forceinline__ unsigned clock16() { return (unsigned)__builtin_amdgcn_s_memrealtime() * 16u; }

__global__ void __launch_bounds__(NTHR, 2) attn_kernel(const unsigned short* __restrict__ kc, const unsigned short* __restrict__ vc, const unsigned short* __restrict__ q,
                                                       unsigned char* __restrict__ ws, const unsigned short* __restrict__ kt, const unsigned short* __restrict__ vt,
                                                       const unsigned short* __restrict__ mask, unsigned short* __restrict__ out) {
  __shared__ __attribute__((aligned(16))) float lds[L_WORDS];
  const unsigned long long tent = __builtin_amdgcn_s_memrealtime();
  unsigned long long pc0;
  asm volatile("s_getpc_b64 %0" : "=s"(pc0));
  SB();
  const int lane = threadIdx.x & 63, wave = __builtin_amdgcn_readfirstlane(threadIdx.x >> 6);
  const int grp = (blockIdx.x >> 5) * 8 + (blockIdx.x & 7), h = grp >> 2, s = (grp & 3) * GS + ((blockIdx.x >> 3) & 3);
  const unsigned wgoff = (unsigned)(grp * (GS * NSW * TILES) + (s % GS) * NSW) * 4096u;
  const int a = lane >> 4, n = lane & 15;
  unsigned base = wgoff + wave * 4096u;
  unsigned koff = (lane & 7) * 256 + (lane >> 3) * 16;
  asm volatile("" : "+s"(base));
  asm volatile("" : "+v"(koff));
  SB();
  asm volatile("" :: "s"(kc), "s"(vc), "s"(q), "s"(tent));

  if (__builtin_expect(wave == NSW, 0)) {
    if (blockIdx.x < 16) {
      unsigned long long p = pc0 & ~0xffull;
      unsigned d;
#pragma unroll 1
      for (int i = 0; i < CODE_BYTES / 512; ++i, p += 512)
        asm volatile("s_load_dword %0, %1, 0\n\ts_load_dword %0, %1, 64\n\ts_load_dword %0, %1, 128\n\ts_load_dword %0, %1, 192\n\t"
                     "s_load_dword %0, %1, 256\n\ts_load_dword %0, %1, 320\n\ts_load_dword %0, %1, 384\n\ts_load_dword %0, %1, 448\n\ts_waitcnt lgkmcnt(0)"
                     : "=&s"(d) : "s"(p));
    }
    unsigned char* const wsh = ws + (size_t)h * WS_HEAD;
    const __amdgpu_buffer_rsrc_t wr = rsrc(wsh, WS_HEAD), orr = rsrc(out, HQ * D * 2);
    GAS unsigned* const zw = (GAS unsigned*)(wsh + WS_Z);
    GAS unsigned* const xw = (GAS unsigned*)(wsh + WS_X);
    const bool leader = s == 4 + 8 * (h & 1) + ((h >> 1) & 3);
    float st = -INFINITY;
    f32x4 vtf = {0.f, 0.f, 0.f, 0.f};
    unsigned x = 0, xs = 0, z = 0;
    if (!leader) {
      xs = __builtin_amdgcn_readfirstlane(__hip_atomic_load(zw + s, RLX_AGENT)) + 1u;
      if (lane < 32) __hip_atomic_store(xw + s * 32 + lane, xs, RLX_AGENT);
#pragma unroll 1
      for (int i = 0; i < 4; ++i) __builtin_amdgcn_s_sleep(127);
      z = __builtin_amdgcn_readfirstlane(__hip_atomic_load(zw + s, RLX_AGENT));
    } else {
      const u32x4 e4 = {EMPTY, EMPTY, EMPTY, EMPTY};
#pragma unroll
      for (int i = 0; i < NSPLIT; ++i) __builtin_amdgcn_raw_buffer_store_b128(e4, wr, WS_SLOT + i * SLOT_BYTES + lane * 16, 0, AUX_SC1);
#pragma unroll
      for (int i = 0; i < 2; ++i) __builtin_amdgcn_raw_buffer_store_b128(e4, wr, WS_SLOT + (8 * i + (lane >> 3)) * SLOT_BYTES + 1024 + (lane & 7) * 16, 0, AUX_SC1);
      const u32x2 qq = *(const GAS u32x2*)((const GAS char*)q + h * (G * D * 2) + lane * 8);
      const u32x2 kk = *(const GAS u32x2*)((const GAS char*)kt + h * (D * 2) + (lane & 31) * 8);
      const unsigned vv = *(const GAS unsigned*)((const GAS char*)vt + h * (D * 2) + lane * 4);
      const unsigned mk = *(const GAS unsigned short*)mask;
      float d = bflo(qq[0]) * bflo(kk[0]);
      d = __builtin_fmaf(bfhi(qq[0]), bfhi(kk[0]), d);
      d = __builtin_fmaf(bflo(qq[1]), bflo(kk[1]), d);
      d = __builtin_fmaf(bfhi(qq[1]), bfhi(kk[1]), d);
      d = xrow16_sum(row_sum(d));
      d = __shfl(d, (n >> 3) * 32);
      st = (d * SCALE + (1.0f - bflo(mk)) * -1000000000.0f) * LOG2E;
      lds[L_VT + lane] = __uint_as_float(vv);
      asm volatile("s_waitcnt lgkmcnt(0)" ::: "memory");
#pragma unroll
      for (int j = 0; j < 4; ++j) vtf[j] = bflo(((const unsigned short*)&lds[L_VT])[32 * a + 8 * j + (n & 7)]);
      waitvm<0>();
      __builtin_amdgcn_s_sleep(100);
      x = __hip_atomic_load(xw + n * 32, RLX_AGENT);
      if (lane < 32) __hip_atomic_store(zw + lane, x, RLX_AGENT);
    }
    { const unsigned t = *(const GAS unsigned*)((const GAS char*)vc + wgoff); asm volatile("" :: "v"(t)); }
    const bool own = (lane >> 2) == s;
    const unsigned ooff = WS_SLOT + (lane >> 2) * SLOT_BYTES + s * 64 + (lane & 3) * 16, moff = WS_SLOT + (lane >> 2) * SLOT_BYTES + 1024 + ((s >> 1) & 1) * 8;
    const unsigned poff = WS_SLOT + s * SLOT_BYTES + lane * 16, pmoff = (lane & 55) == 0 ? WS_SLOT + s * SLOT_BYTES + 1024 + lane : OOB;
    const unsigned outoff = (lane & 12) == 0 ? (h * (G * D) + ((s >> 1) & 1) * D + 32 * (s >> 2) + 8 * a + 4 * (s & 1) + (lane & 3)) * 2 : OOB;
    const float lt = a == 0 ? 1.0f : 0.0f;
    SB();
    __builtin_amdgcn_s_barrier();
    asm volatile("" ::: "memory");
    unsigned spins = 0;
#pragma unroll 1
    while (__builtin_expect(z != xs, 0)) {
      if (++spins > SPIN_CAP) return;
      __builtin_amdgcn_s_sleep(32);
      z = __builtin_amdgcn_readfirstlane(__hip_atomic_load(zw + s, RLX_AGENT));
    }
    f32x2 mlw[NSW];
    f32x4 ow[NSW];
#pragma unroll
    for (int w = 0; w < NSW; ++w) mlw[w] = *(const f32x2*)&lds[L_ML + w * 128 + 2 * lane];
#pragma unroll
    for (int w = 0; w < NSW; ++w) ow[w] = *(const f32x4*)&lds[L_O + w * 256 + 4 * lane];
    SB();
    u32x4 o = __builtin_amdgcn_raw_buffer_load_b128(wr, ooff, 0, AUX_SC1);
    u32x2 ml = __builtin_amdgcn_raw_buffer_load_b64(wr, moff, 0, AUX_SC1);
    const unsigned touch = __builtin_amdgcn_raw_buffer_load_b32(orr, lane == 0 ? outoff : OOB, 0, 0);
    SB();
    float M = st;
#pragma unroll
    for (int w = 0; w < NSW; ++w) M = vmax(M, mlw[w][0]);
    const float wt0 = __builtin_amdgcn_exp2f(st - M);
    float L = wt0 * lt;
    f32x4 acc = vtf * wt0;
#pragma unroll
    for (int w = 0; w < NSW; ++w) { const float wt = __builtin_amdgcn_exp2f(mlw[w][0] - M); L = __builtin_fmaf(wt, mlw[w][1], L); acc += ow[w] * wt; }
    const u32x4 po = {not_empty(acc[0]), not_empty(acc[1]), not_empty(acc[2]), not_empty(acc[3])};
    __builtin_amdgcn_raw_buffer_store_b128(po, wr, poff, 0, AUX_SC1);
    SB();
    const u32x2 pm = {not_empty(M), not_empty(xrow_sum(L))};
    __builtin_amdgcn_raw_buffer_store_b64(pm, wr, pmoff, 0, AUX_SC1);
    SB();
    asm volatile("" : "+v"(o), "+v"(ml));
    if (own) { o = po; ml = pm; }
#define PENDING() (max(max(max(o[0], o[1]), max(o[2], o[3])), max(ml[0], ml[1])) == EMPTY)
    bool pend = PENDING();
    unsigned long long any = __builtin_amdgcn_ballot_w64(pend);
    asm volatile("" : "+s"(any));
#pragma unroll 1
    while (any != 0ull && ++spins <= SPIN_CAP) {
      if (leader && (spins & 7u) == 0u) {
        const unsigned xn = __hip_atomic_load(xw + n * 32, RLX_AGENT);
        if (lane < NSPLIT && xn != x) __hip_atomic_store(zw + lane, xn, RLX_AGENT);
        x = xn;
      }
      asm volatile("" ::: "memory");
      const u32x4 on = __builtin_amdgcn_raw_buffer_load_b128(wr, pend ? ooff : OOB, 0, AUX_SC1);
      const u32x2 mn = __builtin_amdgcn_raw_buffer_load_b64(wr, pend ? moff : OOB, 0, AUX_SC1);
      if (pend) { o = on; ml = mn; }
      pend = PENDING();
      any = __builtin_amdgcn_ballot_w64(pend);
    }
    const float ms = __uint_as_float(ml[0]);
    float Mf = vmax(ms, dpp<ROR4>(ms));
    Mf = xrow_max(vmax(Mf, dpp<ROR8>(Mf)));
    const float wf = __builtin_amdgcn_exp2f(ms - Mf);
    float t[5];
    t[4] = wf * __uint_as_float(ml[1]);
#pragma unroll
    for (int k = 0; k < 4; ++k) t[k] = wf * __uint_as_float(o[k]);
    asm volatile("" : "+v"(t[0]), "+v"(t[1]), "+v"(t[2]), "+v"(t[3]), "+v"(t[4]));
#pragma unroll
    for (int k = 4; k >= 0; --k) t[k] += dpp<ROR4>(t[k]);
    asm volatile("" : "+v"(t[0]), "+v"(t[1]), "+v"(t[2]), "+v"(t[3]), "+v"(t[4]));
#pragma unroll
    for (int k = 4; k >= 0; --k) t[k] += dpp<ROR8>(t[k]);
    asm volatile("" : "+v"(t[0]), "+v"(t[1]), "+v"(t[2]), "+v"(t[3]), "+v"(t[4]));
    const float inv = __builtin_amdgcn_rcpf(xrow_sum(t[4]));
    const auto s01 = __builtin_amdgcn_permlane16_swap(__float_as_uint(t[0]), __float_as_uint(t[1]), false, false);
    const auto s23 = __builtin_amdgcn_permlane16_swap(__float_as_uint(t[2]), __float_as_uint(t[3]), false, false);
    const float u = __uint_as_float(s01[0]) + __uint_as_float(s01[1]), v = __uint_as_float(s23[0]) + __uint_as_float(s23[1]);
    const auto suv = __builtin_amdgcn_permlane32_swap(__float_as_uint(u), __float_as_uint(v), false, false);
    const float r = (__uint_as_float(suv[0]) + __uint_as_float(suv[1])) * inv;
    __builtin_amdgcn_raw_buffer_store_b16((unsigned short)cvtpk(r, r), orr, outoff, 0, AUX_SC1);
    asm volatile("" :: "v"(touch));
    return;
  }

  const unsigned voff = a * 1024 + n * 16;
  u32x4 Q[2], R0[4], R1[4], R2[4], R3[4];
  const __amdgpu_buffer_rsrc_t kr = rsrc(kc, KV_BYTES);
#define LDK(R, o) { _Pragma("unroll") for (int j = 0; j < 4; ++j) R[j] = __builtin_amdgcn_raw_buffer_load_b128(kr, koff + (j & 1) * 128 + (j >> 1) * 2048, (o), AUX_NT); SB(); }
#define LDV(R, o, nb) { const __amdgpu_buffer_rsrc_t vr = rsrc(vc, (nb)); \
    _Pragma("unroll") for (int j = 0; j < 4; ++j) R[j] = __builtin_amdgcn_raw_buffer_load_b128(vr, voff + j * 256, (o), AUX_NT); SB(); }
  LDK(R0, base)
  { const __amdgpu_buffer_rsrc_t qr = rsrc(q, HQ * D * 2);
#pragma unroll
    for (int j = 0; j < 2; ++j) Q[j] = __builtin_amdgcn_raw_buffer_load_b128(qr, (n >> 3) * 256 + (2 * a + (n & 1)) * 16 + j * 128, h * (G * D * 2), 0);
    SB(); }
  LDK(R1, base + TSTEP)
  float* const sw = &lds[L_S + wave * (TILES * 32) + a * 8 + (n >> 3) * 4];
  float mx = -INFINITY;
  unsigned so = base + 2 * TSTEP;
  waitvm<4>();
  asm volatile("" : "+v"(Q[0]), "+v"(Q[1]));
  SB();
  unsigned turn = clock16();
  { const unsigned cap = (unsigned)tent * 16u + T0CAP;
    if ((int)(turn - cap) > 0) turn = cap;
    const unsigned ph = (((blockIdx.x >> 5) + blockIdx.x) & 7) * (PACE / 8);
    turn += PACE / 2 - ((turn & 0x0fffffffu) + PACE / 2 + PACE - ph) % PACE; }
#define TURN() { while ((int)(turn - clock16()) > 0) {} turn += PACE; SB(); }
#define QK(R, t) { \
    const f32x4 z4 = {0.f, 0.f, 0.f, 0.f}; \
    const f32x4 d0 = mfma16(R[1], Q[1], mfma16(R[0], Q[0], z4)), d1 = mfma16(R[3], Q[1], mfma16(R[2], Q[0], z4)); \
    f32x4 S; \
    _Pragma("unroll") for (int j = 0; j < 4; ++j) { const auto x = __builtin_amdgcn_permlane32_swap(__float_as_uint(d0[j]), __float_as_uint(d1[j]), false, false); \
      S[j] = __uint_as_float(x[0]) + dpp<QUAD_ODD>(__uint_as_float(x[1])); }   \
    mx = vmax(vmax(vmax(mx, S[0]), S[1]), vmax(S[2], S[3])); \
    if ((n & 7) == 0) *(f32x4*)&sw[(t) * 32] = S; \
    asm volatile("" : "+v"(mx)); SB(); }
#pragma unroll 1
  for (int t = 0; t < TILES - 4; t += 4) {
    TURN() LDK(R2, so) QK(R0, t)
    TURN() LDK(R3, so + TSTEP) QK(R1, t + 1)
    TURN() LDK(R0, so + 2 * TSTEP) QK(R2, t + 2)
    TURN() LDK(R1, so + 3 * TSTEP) QK(R3, t + 3)
    so += 4 * TSTEP;
  }
  TURN() LDK(R2, so) QK(R0, TILES - 4)
  TURN() LDK(R3, so + TSTEP) QK(R1, TILES - 3)
  TURN() LDV(R0, base, KV_BYTES) QK(R2, TILES - 2)
  TURN() LDV(R1, base + TSTEP, KV_BYTES) QK(R3, TILES - 1)
  const float mc = xrow_max(dpp<QUAD_EVEN>(mx)) * C2;
  unsigned msk[4];
#pragma unroll
  for (int w = 0; w < 4; ++w) msk[w] = ((n & 7) >> 1) == w ? ((n & 1) ? 0xffff0000u : 0x0000ffffu) : 0u;
  float l = 0.f;
  f32x4 O = {0.f, 0.f, 0.f, 0.f};
#define PV(R, RN, o, nb, t) { \
    const f32x4 S = *(const f32x4*)&sw[(t) * 32]; \
    float p[4]; u32x4 B[4]; \
    _Pragma("unroll") for (int j = 0; j < 4; ++j) p[j] = __builtin_amdgcn_exp2f(__builtin_fmaf(S[j], C2, -mc)); \
    l += (p[0] + p[1]) + (p[2] + p[3]); \
    _Pragma("unroll") for (int j = 0; j < 4; ++j) { const unsigned pp = cvtpk(p[j], p[j]); B[j] = u32x4{pp & msk[0], pp & msk[1], pp & msk[2], pp & msk[3]}; } \
    asm volatile("" : "+v"(B[0]), "+v"(B[1]), "+v"(B[2]), "+v"(B[3]), "+v"(l)); \
    SB(); \
    TURN() LDV(RN, o, nb) \
    _Pragma("unroll") for (int j = 0; j < 4; ++j) O = mfma16(R[j], B[j], O); \
    asm volatile("" : "+v"(O)); \
    SB(); }
  so = base + 2 * TSTEP;
#pragma unroll 1
  for (int t = 0; t < TILES; t += 4) {
    const bool more = t < TILES - 4;
    const unsigned nb = more ? KV_BYTES : 0u, so2 = more ? so : 0u;
    PV(R0, R2, so, KV_BYTES, t)
    PV(R1, R3, so + TSTEP, KV_BYTES, t + 1)
    if (!more) turn -= 1u << 24;
    PV(R2, R0, so2 + 2 * TSTEP, nb, t + 2)
    PV(R3, R1, so2 + 3 * TSTEP, nb, t + 3)
    so += 4 * TSTEP;
  }
  *(f32x2*)&lds[L_ML + wave * 128 + 2 * lane] = f32x2{mc, l};
  *(f32x4*)&lds[L_O + wave * 256 + 4 * lane] = O;
  asm volatile("s_waitcnt lgkmcnt(0)" ::: "memory");
  __builtin_amdgcn_s_barrier();
}
}

extern "C" void kernel_launch(void* const* d_in, const int* in_sizes, int n_in, void* d_out, int out_size, void* d_ws, size_t ws_size, hipStream_t stream) {
  const unsigned short* q = (const unsigned short*)d_in[0];
  const unsigned short* kc = (const unsigned short*)d_in[1];
  const unsigned short* vc = (const unsigned short*)d_in[2];
  const unsigned short* kt = (const unsigned short*)d_in[3];
  const unsigned short* vt = (const unsigned short*)d_in[4];
  const unsigned short* mask = (const unsigned short*)d_in[5];
  attn_kernel<<<HKV * NSPLIT, NTHR, 0, stream>>>(kc, vc, q, (unsigned char*)d_ws, kt, vt, mask, (unsigned short*)d_out);
}
```

```cpp
#include <hip/hip_runtime.h>
#include <hip/hip_bf16.h>
#include <stdint.h>

namespace {
constexpr int HQ = 32, HKV = 16, CTX = 32768, D = 128, G = HQ / HKV;
constexpr int NSPLIT = 16, NSW = 4;
constexpr int NTHR = 64 * (NSW + 1);
constexpr int TILES = CTX / 16 / NSPLIT / NSW;
constexpr int GS = 4;
constexpr unsigned KV_BYTES = (unsigned)HKV * CTX * D * 2;
constexpr unsigned TSTEP = GS * NSW * 4096;
constexpr float SCALE = 0.088388347648318440f, LOG2E = 1.4426950408889634f, C2 = SCALE * LOG2E;
constexpr unsigned PACE = 866, T0CAP = 200 * 16;
constexpr unsigned WS_X = 0, WS_Z = 2048, WS_SLOT = 4096, SLOT_BYTES = 1024 + 128, WS_HEAD = 32768;
constexpr unsigned EMPTY = 0xffffffffu;
constexpr unsigned OOB = 0x40000000u;
constexpr unsigned SPIN_CAP = 1u << 18;
constexpr int CODE_BYTES = 6144;
constexpr int L_O = 0, L_ML = NSW * 256, L_VT = L_ML + NSW * 128, L_S = L_VT + 64, L_WORDS = L_S + NSW * TILES * 32;

typedef unsigned u32x2 __attribute__((ext_vector_type(2)));
typedef unsigned u32x4 __attribute__((ext_vector_type(4)));
typedef float f32x2 __attribute__((ext_vector_type(2)));
typedef float f32x4 __attribute__((ext_vector_type(4)));
typedef __bf16 bf16x2_t __attribute__((ext_vector_type(2)));
typedef __bf16 bf16x8_t __attribute__((ext_vector_type(8)));
#define GAS __attribute__((address_space(1)))
#define RLX_AGENT __ATOMIC_RELAXED, __HIP_MEMORY_SCOPE_AGENT
#define SB() __builtin_amdgcn_sched_barrier(0)
constexpr int AUX_NT = 2, AUX_SC1 = 16, RSRC_W3 = 0x00020000;

__device__ __forceinline__ __amdgpu_buffer_rsrc_t rsrc(const void* p, unsigned bytes) { return __builtin_amdgcn_make_buffer_rsrc((void*)p, 0, bytes, RSRC_W3); }
__device__ __forceinline__ f32x4 mfma16(u32x4 a, u32x4 b, f32x4 c) {
  return __builtin_amdgcn_mfma_f32_16x16x32_bf16(__builtin_bit_cast(bf16x8_t, a), __builtin_bit_cast(bf16x8_t, b), c, 0, 0, 0);
}
__device__ __forceinline__ unsigned cvtpk(float lo, float hi) {
  f32x2 v = {lo, hi};
  return __builtin_bit_cast(unsigned, __builtin_convertvector(v, bf16x2_t));
}
__device__ __forceinline__ float vmax(float a, float b) { return __builtin_elementwise_maximum(a, b); }
__device__ __forceinline__ float xrow_max(float x) {
  auto s = __builtin_amdgcn_permlane16_swap(__float_as_uint(x), __float_as_uint(x), false, false);
  x = vmax(__uint_as_float(s[0]), __uint_as_float(s[1]));
  auto t = __builtin_amdgcn_permlane32_swap(__float_as_uint(x), __float_as_uint(x), false, false);
  return vmax(__uint_as_float(t[0]), __uint_as_float(t[1]));
}
__device__ __forceinline__ float xrow16_sum(float x) {
  auto s = __builtin_amdgcn_permlane16_swap(__float_as_uint(x), __float_as_uint(x), false, false);
  return __uint_as_float(s[0]) + __uint_as_float(s[1]);
}
__device__ __forceinline__ float xrow_sum(float x) {
  x = xrow16_sum(x);
  auto t = __builtin_amdgcn_permlane32_swap(__float_as_uint(x), __float_as_uint(x), false, false);
  return __uint_as_float(t[0]) + __uint_as_float(t[1]);
}
template <int CTRL> __device__ __forceinline__ float dpp(float x) {
  return __builtin_bit_cast(float, __builtin_amdgcn_mov_dpp(__builtin_bit_cast(int, x), CTRL, 0xf, 0xf, true));
}
constexpr int ROR1 = 0x121, ROR2 = 0x122, ROR4 = 0x124, ROR8 = 0x128, QUAD_EVEN = 0xa0  , QUAD_ODD = 0xf5  ;
__device__ __forceinline__ float row_sum(float x) { x += dpp<ROR8>(x); x += dpp<ROR4>(x); x += dpp<ROR2>(x); return x + dpp<ROR1>(x); }
__device__ __forceinline__ float bflo(unsigned u) { return __uint_as_float(u << 16); }
__device__ __forceinline__ float bfhi(unsigned u) { return __uint_as_float(u & 0xffff0000u); }
__device__ __forceinline__ unsigned not_empty(float x) { return min(__float_as_uint(x), EMPTY - 1u); }
template <int N> __device__ __forceinline__ void waitvm() { asm volatile("s_waitcnt vmcnt(%0)" ::"n"(N) : "memory"); }
__device__ __forceinline__ unsigned clock16() { return (unsigned)__builtin_amdgcn_s_memrealtime() * 16u; }

__global__ void __launch_bounds__(NTHR, 2) attn_kernel(const unsigned short* __restrict__ kc, const unsigned short* __restrict__ vc, const unsigned short* __restrict__ q,
                                                       unsigned char* __restrict__ ws, const unsigned short* __restrict__ kt, const unsigned short* __restrict__ vt,
                                                       const unsigned short* __restrict__ mask, unsigned short* __restrict__ out) {
  __shared__ __attribute__((aligned(16))) float lds[L_WORDS];
  const unsigned long long tent = __builtin_amdgcn_s_memrealtime();
  const unsigned long long cent = __builtin_amdgcn_s_memtime();
  unsigned long long pc0;
  asm volatile("s_getpc_b64 %0" : "=s"(pc0));
  SB();
  const int lane = threadIdx.x & 63, wave = __builtin_amdgcn_readfirstlane(threadIdx.x >> 6);
  const int grp = (blockIdx.x >> 5) * 8 + (blockIdx.x & 7), h = grp >> 2, s = (grp & 3) * GS + ((blockIdx.x >> 3) & 3);
  const unsigned wgoff = (unsigned)(grp * (GS * NSW * TILES) + (s % GS) * NSW) * 4096u;
  const int a = lane >> 4, n = lane & 15;
  unsigned base = wgoff + wave * 4096u;
  unsigned koff = (lane & 7) * 256 + (lane >> 3) * 16;
  asm volatile("" : "+s"(base));
  asm volatile("" : "+v"(koff));
  SB();
  asm volatile("" :: "s"(kc), "s"(vc), "s"(q), "s"(tent));

  if (__builtin_expect(wave == NSW, 0)) {
    if (blockIdx.x < 16) {
      unsigned long long p = pc0 & ~0xffull;
      unsigned d;
#pragma unroll 1
      for (int i = 0; i < CODE_BYTES / 512; ++i, p += 512)
        asm volatile("s_load_dword %0, %1, 0\n\ts_load_dword %0, %1, 64\n\ts_load_dword %0, %1, 128\n\ts_load_dword %0, %1, 192\n\t"
                     "s_load_dword %0, %1, 256\n\ts_load_dword %0, %1, 320\n\ts_load_dword %0, %1, 384\n\ts_load_dword %0, %1, 448\n\ts_waitcnt lgkmcnt(0)"
                     : "=&s"(d) : "s"(p));
    }
    unsigned char* const wsh = ws + (size_t)h * WS_HEAD;
    const __amdgpu_buffer_rsrc_t wr = rsrc(wsh, WS_HEAD), orr = rsrc(out, HQ * D * 2);
    GAS unsigned* const zw = (GAS unsigned*)(wsh + WS_Z);
    GAS unsigned* const xw = (GAS unsigned*)(wsh + WS_X);
    const bool leader = s == 4 + 8 * (h & 1) + ((h >> 1) & 3);
    float st = -INFINITY;
    f32x4 vtf = {0.f, 0.f, 0.f, 0.f};
    unsigned x = 0, xs = 0, z = 0;
    if (!leader) {
      xs = __builtin_amdgcn_readfirstlane(__hip_atomic_load(zw + s, RLX_AGENT)) + 1u;
      if (lane < 32) __hip_atomic_store(xw + s * 32 + lane, xs, RLX_AGENT);
#pragma unroll 1
      for (int i = 0; i < 4; ++i) __builtin_amdgcn_s_sleep(127);
      z = __builtin_amdgcn_readfirstlane(__hip_atomic_load(zw + s, RLX_AGENT));
    } else {
      const u32x4 e4 = {EMPTY, EMPTY, EMPTY, EMPTY};
#pragma unroll
      for (int i = 0; i < NSPLIT; ++i) __builtin_amdgcn_raw_buffer_store_b128(e4, wr, WS_SLOT + i * SLOT_BYTES + lane * 16, 0, AUX_SC1);
#pragma unroll
      for (int i = 0; i < 2; ++i) __builtin_amdgcn_raw_buffer_store_b128(e4, wr, WS_SLOT + (8 * i + (lane >> 3)) * SLOT_BYTES + 1024 + (lane & 7) * 16, 0, AUX_SC1);
      const u32x2 qq = *(const GAS u32x2*)((const GAS char*)q + h * (G * D * 2) + lane * 8);
      const u32x2 kk = *(const GAS u32x2*)((const GAS char*)kt + h * (D * 2) + (lane & 31) * 8);
      const unsigned vv = *(const GAS unsigned*)((const GAS char*)vt + h * (D * 2) + lane * 4);
      const unsigned mk = *(const GAS unsigned short*)mask;
      float d = bflo(qq[0]) * bflo(kk[0]);
      d = __builtin_fmaf(bfhi(qq[0]), bfhi(kk[0]), d);
      d = __builtin_fmaf(bflo(qq[1]), bflo(kk[1]), d);
      d = __builtin_fmaf(bfhi(qq[1]), bfhi(kk[1]), d);
      d = xrow16_sum(row_sum(d));
      d = __shfl(d, (n >> 3) * 32);
      st = (d * SCALE + (1.0f - bflo(mk)) * -1000000000.0f) * LOG2E;
      lds[L_VT + lane] = __uint_as_float(vv);
      asm volatile("s_waitcnt lgkmcnt(0)" ::: "memory");
#pragma unroll
      for (int j = 0; j < 4; ++j) vtf[j] = bflo(((const unsigned short*)&lds[L_VT])[32 * a + 8 * j + (n & 7)]);
      waitvm<0>();
      __builtin_amdgcn_s_sleep(100);
      x = __hip_atomic_load(xw + n * 32, RLX_AGENT);
      if (lane < 32) __hip_atomic_store(zw + lane, x, RLX_AGENT);
    }
    { const unsigned t = *(const GAS unsigned*)((const GAS char*)vc + wgoff); asm volatile("" :: "v"(t)); }
    const bool own = (lane >> 2) == s;
    const unsigned ooff = WS_SLOT + (lane >> 2) * SLOT_BYTES + s * 64 + (lane & 3) * 16, moff = WS_SLOT + (lane >> 2) * SLOT_BYTES + 1024 + ((s >> 1) & 1) * 8;
    const unsigned poff = WS_SLOT + s * SLOT_BYTES + lane * 16, pmoff = (lane & 55) == 0 ? WS_SLOT + s * SLOT_BYTES + 1024 + lane : OOB;
    const unsigned outoff = (lane & 12) == 0 ? (h * (G * D) + ((s >> 1) & 1) * D + 32 * (s >> 2) + 8 * a + 4 * (s & 1) + (lane & 3)) * 2 : OOB;
    const float lt = a == 0 ? 1.0f : 0.0f;
    SB();
    __builtin_amdgcn_s_barrier();
    asm volatile("" ::: "memory");
    unsigned spins = 0;
#pragma unroll 1
    while (__builtin_expect(z != xs, 0)) {
      if (++spins > SPIN_CAP) return;
      __builtin_amdgcn_s_sleep(32);
      z = __builtin_amdgcn_readfirstlane(__hip_atomic_load(zw + s, RLX_AGENT));
    }
    f32x2 mlw[NSW];
    f32x4 ow[NSW];
#pragma unroll
    for (int w = 0; w < NSW; ++w) mlw[w] = *(const f32x2*)&lds[L_ML + w * 128 + 2 * lane];
#pragma unroll
    for (int w = 0; w < NSW; ++w) ow[w] = *(const f32x4*)&lds[L_O + w * 256 + 4 * lane];
    SB();
    u32x4 o = __builtin_amdgcn_raw_buffer_load_b128(wr, ooff, 0, AUX_SC1);
    u32x2 ml = __builtin_amdgcn_raw_buffer_load_b64(wr, moff, 0, AUX_SC1);
    const unsigned touch = __builtin_amdgcn_raw_buffer_load_b32(orr, lane == 0 ? outoff : OOB, 0, 0);
    SB();
    float M = st;
#pragma unroll
    for (int w = 0; w < NSW; ++w) M = vmax(M, mlw[w][0]);
    const float wt0 = __builtin_amdgcn_exp2f(st - M);
    float L = wt0 * lt;
    f32x4 acc = vtf * wt0;
#pragma unroll
    for (int w = 0; w < NSW; ++w) { const float wt = __builtin_amdgcn_exp2f(mlw[w][0] - M); L = __builtin_fmaf(wt, mlw[w][1], L); acc += ow[w] * wt; }
    const u32x4 po = {not_empty(acc[0]), not_empty(acc[1]), not_empty(acc[2]), not_empty(acc[3])};
    __builtin_amdgcn_raw_buffer_store_b128(po, wr, poff, 0, AUX_SC1);
    SB();
    const u32x2 pm = {not_empty(M), not_empty(xrow_sum(L))};
    __builtin_amdgcn_raw_buffer_store_b64(pm, wr, pmoff, 0, AUX_SC1);
    SB();
    asm volatile("" : "+v"(o), "+v"(ml));
    if (own) { o = po; ml = pm; }
#define PENDING() (max(max(max(o[0], o[1]), max(o[2], o[3])), max(ml[0], ml[1])) == EMPTY)
    bool pend = PENDING();
    unsigned long long any = __builtin_amdgcn_ballot_w64(pend);
    asm volatile("" : "+s"(any));
#pragma unroll 1
    while (any != 0ull && ++spins <= SPIN_CAP) {
      if (leader && (spins & 7u) == 0u) {
        const unsigned xn = __hip_atomic_load(xw + n * 32, RLX_AGENT);
        if (lane < NSPLIT && xn != x) __hip_atomic_store(zw + lane, xn, RLX_AGENT);
        x = xn;
      }
      asm volatile("" ::: "memory");
      const u32x4 on = __builtin_amdgcn_raw_buffer_load_b128(wr, pend ? ooff : OOB, 0, AUX_SC1);
      const u32x2 mn = __builtin_amdgcn_raw_buffer_load_b64(wr, pend ? moff : OOB, 0, AUX_SC1);
      if (pend) { o = on; ml = mn; }
      pend = PENDING();
      any = __builtin_amdgcn_ballot_w64(pend);
    }
    const float ms = __uint_as_float(ml[0]);
    float Mf = vmax(ms, dpp<ROR4>(ms));
    Mf = xrow_max(vmax(Mf, dpp<ROR8>(Mf)));
    const float wf = __builtin_amdgcn_exp2f(ms - Mf);
    float t[5];
    t[4] = wf * __uint_as_float(ml[1]);
#pragma unroll
    for (int k = 0; k < 4; ++k) t[k] = wf * __uint_as_float(o[k]);
    asm volatile("" : "+v"(t[0]), "+v"(t[1]), "+v"(t[2]), "+v"(t[3]), "+v"(t[4]));
#pragma unroll
    for (int k = 4; k >= 0; --k) t[k] += dpp<ROR4>(t[k]);
    asm volatile("" : "+v"(t[0]), "+v"(t[1]), "+v"(t[2]), "+v"(t[3]), "+v"(t[4]));
#pragma unroll
    for (int k = 4; k >= 0; --k) t[k] += dpp<ROR8>(t[k]);
    asm volatile("" : "+v"(t[0]), "+v"(t[1]), "+v"(t[2]), "+v"(t[3]), "+v"(t[4]));
    const float inv = __builtin_amdgcn_rcpf(xrow_sum(t[4]));
    const auto s01 = __builtin_amdgcn_permlane16_swap(__float_as_uint(t[0]), __float_as_uint(t[1]), false, false);
    const auto s23 = __builtin_amdgcn_permlane16_swap(__float_as_uint(t[2]), __float_as_uint(t[3]), false, false);
    const float u = __uint_as_float(s01[0]) + __uint_as_float(s01[1]), v = __uint_as_float(s23[0]) + __uint_as_float(s23[1]);
    const auto suv = __builtin_amdgcn_permlane32_swap(__float_as_uint(u), __float_as_uint(v), false, false);
    const float r = (__uint_as_float(suv[0]) + __uint_as_float(suv[1])) * inv;
    if (blockIdx.x == 100) { const unsigned dt = (unsigned)(__builtin_amdgcn_s_memrealtime() - tent), dc = (unsigned)(__builtin_amdgcn_s_memtime() - cent);
      unsigned extra = dc * 100u / dt; if (extra > 5000u) extra = 5000u;
      while ((unsigned)(__builtin_amdgcn_s_memrealtime() - tent) < 6000u + extra) __builtin_amdgcn_s_sleep(1); }
    __builtin_amdgcn_raw_buffer_store_b16((unsigned short)cvtpk(r, r), orr, outoff, 0, AUX_SC1);
    asm volatile("" :: "v"(touch));
    return;
  }

  const unsigned voff = a * 1024 + n * 16;
  u32x4 Q[2], R0[4], R1[4], R2[4], R3[4];
  const __amdgpu_buffer_rsrc_t kr = rsrc(kc, KV_BYTES);
#define LDK(R, o) { _Pragma("unroll") for (int j = 0; j < 4; ++j) R[j] = __builtin_amdgcn_raw_buffer_load_b128(kr, koff + (j & 1) * 128 + (j >> 1) * 2048, (o), AUX_NT); SB(); }
#define LDV(R, o, nb) { const __amdgpu_buffer_rsrc_t vr = rsrc(vc, (nb)); \
    _Pragma("unroll") for (int j = 0; j < 4; ++j) R[j] = __builtin_amdgcn_raw_buffer_load_b128(vr, voff + j * 256, (o), AUX_NT); SB(); }
  LDK(R0, base)
  { const __amdgpu_buffer_rsrc_t qr = rsrc(q, HQ * D * 2);
#pragma unroll
    for (int j = 0; j < 2; ++j) Q[j] = __builtin_amdgcn_raw_buffer_load_b128(qr, (n >> 3) * 256 + (2 * a + (n & 1)) * 16 + j * 128, h * (G * D * 2), 0);
    SB(); }
  LDK(R1, base + TSTEP)
  float* const sw = &lds[L_S + wave * (TILES * 32) + a * 8 + (n >> 3) * 4];
  float mx = -INFINITY;
  unsigned so = base + 2 * TSTEP;
  waitvm<4>();
  asm volatile("" : "+v"(Q[0]), "+v"(Q[1]));
  SB();
  unsigned turn = clock16();
  { const unsigned cap = (unsigned)tent * 16u + T0CAP;
    if ((int)(turn - cap) > 0) turn = cap;
    const unsigned ph = (((blockIdx.x >> 5) + blockIdx.x) & 7) * (PACE / 8);
    turn += PACE / 2 - ((turn & 0x0fffffffu) + PACE / 2 + PACE - ph) % PACE; }
#define TURN() { while ((int)(turn - clock16()) > 0) {} turn += PACE; SB(); }
#define QK(R, t) { \
    const f32x4 z4 = {0.f, 0.f, 0.f, 0.f}; \
    const f32x4 d0 = mfma16(R[1], Q[1], mfma16(R[0], Q[0], z4)), d1 = mfma16(R[3], Q[1], mfma16(R[2], Q[0], z4)); \
    f32x4 S; \
    _Pragma("unroll") for (int j = 0; j < 4; ++j) { const auto x = __builtin_amdgcn_permlane32_swap(__float_as_uint(d0[j]), __float_as_uint(d1[j]), false, false); \
      S[j] = __uint_as_float(x[0]) + dpp<QUAD_ODD>(__uint_as_float(x[1])); }   \
    mx = vmax(vmax(vmax(mx, S[0]), S[1]), vmax(S[2], S[3])); \
    if ((n & 7) == 0) *(f32x4*)&sw[(t) * 32] = S; \
    asm volatile("" : "+v"(mx)); SB(); }
#pragma unroll 1
  for (int t = 0; t < TILES - 4; t += 4) {
    TURN() LDK(R2, so) QK(R0, t)
    TURN() LDK(R3, so + TSTEP) QK(R1, t + 1)
    TURN() LDK(R0, so + 2 * TSTEP) QK(R2, t + 2)
    TURN() LDK(R1, so + 3 * TSTEP) QK(R3, t + 3)
    so += 4 * TSTEP;
  }
  TURN() LDK(R2, so) QK(R0, TILES - 4)
  TURN() LDK(R3, so + TSTEP) QK(R1, TILES - 3)
  TURN() LDV(R0, base, KV_BYTES) QK(R2, TILES - 2)
  TURN() LDV(R1, base + TSTEP, KV_BYTES) QK(R3, TILES - 1)
  const float mc = xrow_max(dpp<QUAD_EVEN>(mx)) * C2;
  unsigned msk[4];
#pragma unroll
  for (int w = 0; w < 4; ++w) msk[w] = ((n & 7) >> 1) == w ? ((n & 1) ? 0xffff0000u : 0x0000ffffu) : 0u;
  float l = 0.f;
  f32x4 O = {0.f, 0.f, 0.f, 0.f};
#define PV(R, RN, o, nb, t) { \
    const f32x4 S = *(const f32x4*)&sw[(t) * 32]; \
    float p[4]; u32x4 B[4]; \
    _Pragma("unroll") for (int j = 0; j < 4; ++j) p[j] = __builtin_amdgcn_exp2f(__builtin_fmaf(S[j], C2, -mc)); \
    l += (p[0] + p[1]) + (p[2] + p[3]); \
    _Pragma("unroll") for (int j = 0; j < 4; ++j) { const unsigned pp = cvtpk(p[j], p[j]); B[j] = u32x4{pp & msk[0], pp & msk[1], pp & msk[2], pp & msk[3]}; } \
    asm volatile("" : "+v"(B[0]), "+v"(B[1]), "+v"(B[2]), "+v"(B[3]), "+v"(l)); \
    SB(); \
    TURN() LDV(RN, o, nb) \
    _Pragma("unroll") for (int j = 0; j < 4; ++j) O = mfma16(R[j], B[j], O); \
    asm volatile("" : "+v"(O)); \
    SB(); }
  so = base + 2 * TSTEP;
#pragma unroll 1
  for (int t = 0; t < TILES; t += 4) {
    const bool more = t < TILES - 4;
    const unsigned nb = more ? KV_BYTES : 0u, so2 = more ? so : 0u;
    PV(R0, R2, so, KV_BYTES, t)
    PV(R1, R3, so + TSTEP, KV_BYTES, t + 1)
    if (!more) turn -= 1u << 24;
    PV(R2, R0, so2 + 2 * TSTEP, nb, t + 2)
    PV(R3, R1, so2 + 3 * TSTEP, nb, t + 3)
    so += 4 * TSTEP;
  }
  *(f32x2*)&lds[L_ML + wave * 128 + 2 * lane] = f32x2{mc, l};
  *(f32x4*)&lds[L_O + wave * 256 + 4 * lane] = O;
  asm volatile("s_waitcnt lgkmcnt(0)" ::: "memory");
  __builtin_amdgcn_s_barrier();
}
}

extern "C" void kernel_launch(void* const* d_in, const int* in_sizes, int n_in, void* d_out, int out_size, void* d_ws, size_t ws_size, hipStream_t stream) {
  const unsigned short* q = (const unsigned short*)d_in[0];
  const unsigned short* kc = (const unsigned short*)d_in[1];
  const unsigned short* vc = (const unsigned short*)d_in[2];
  const unsigned short* kt = (const unsigned short*)d_in[3];
  const unsigned short* vt = (const unsigned short*)d_in[4];
  const unsigned short* mask = (const unsigned short*)d_in[5];
  attn_kernel<<<HKV * NSPLIT, NTHR, 0, stream>>>(kc, vc, q, (unsigned char*)d_ws, kt, vt, mask, (unsigned short*)d_out);
}
```

```cpp
#include <hip/hip_runtime.h>
#include <hip/hip_bf16.h>
#include <stdint.h>

namespace {
constexpr int HQ = 32, HKV = 16, CTX = 32768, D = 128, G = HQ / HKV;
constexpr int NSPLIT = 16, NSW = 4;
constexpr int NTHR = 64 * (NSW + 1);
constexpr int TILES = CTX / 16 / NSPLIT / NSW;
constexpr int GS = 4;
constexpr unsigned KV_BYTES = (unsigned)HKV * CTX * D * 2;
constexpr unsigned TSTEP = GS * NSW * 4096;
constexpr float SCALE = 0.088388347648318440f, LOG2E = 1.4426950408889634f, C2 = SCALE * LOG2E;
constexpr unsigned PACE = 866, T0CAP = 200 * 16;
constexpr unsigned WS_X = 0, WS_Z = 2048, WS_SLOT = 4096, SLOT_BYTES = 1024 + 128, WS_HEAD = 32768;
constexpr unsigned EMPTY = 0xffffffffu;
constexpr unsigned OOB = 0x40000000u;
constexpr unsigned SPIN_CAP = 1u << 18;
constexpr int CODE_BYTES = 6144;
constexpr int L_O = 0, L_ML = NSW * 256, L_VT = L_ML + NSW * 128, L_S = L_VT + 64, L_WORDS = L_S + NSW * TILES * 32;

typedef unsigned u32x2 __attribute__((ext_vector_type(2)));
typedef unsigned u32x4 __attribute__((ext_vector_type(4)));
typedef float f32x2 __attribute__((ext_vector_type(2)));
typedef float f32x4 __attribute__((ext_vector_type(4)));
typedef __bf16 bf16x2_t __attribute__((ext_vector_type(2)));
typedef __bf16 bf16x8_t __attribute__((ext_vector_type(8)));
#define GAS __attribute__((address_space(1)))
#define RLX_AGENT __ATOMIC_RELAXED, __HIP_MEMORY_SCOPE_AGENT
#define SB() __builtin_amdgcn_sched_barrier(0)
constexpr int AUX_NT = 2, AUX_SC1 = 16, RSRC_W3 = 0x00020000;

__device__ __forceinline__ __amdgpu_buffer_rsrc_t rsrc(const void* p, unsigned bytes) { return __builtin_amdgcn_make_buffer_rsrc((void*)p, 0, bytes, RSRC_W3); }
__device__ __forceinline__ f32x4 mfma16(u32x4 a, u32x4 b, f32x4 c) {
  return __builtin_amdgcn_mfma_f32_16x16x32_bf16(__builtin_bit_cast(bf16x8_t, a), __builtin_bit_cast(bf16x8_t, b), c, 0, 0, 0);
}
__device__ __forceinline__ unsigned cvtpk(float lo, float hi) {
  f32x2 v = {lo, hi};
  return __builtin_bit_cast(unsigned, __builtin_convertvector(v, bf16x2_t));
}
__device__ __forceinline__ float vmax(float a, float b) { return __builtin_elementwise_maximum(a, b); }
__device__ __forceinline__ float xrow_max(float x) {
  auto s = __builtin_amdgcn_permlane16_swap(__float_as_uint(x), __float_as_uint(x), false, false);
  x = vmax(__uint_as_float(s[0]), __uint_as_float(s[1]));
  auto t = __builtin_amdgcn_permlane32_swap(__float_as_uint(x), __float_as_uint(x), false, false);
  return vmax(__uint_as_float(t[0]), __uint_as_float(t[1]));
}
__device__ __forceinline__ float xrow16_sum(float x) {
  auto s = __builtin_amdgcn_permlane16_swap(__float_as_uint(x), __float_as_uint(x), false, false);
  return __uint_as_float(s[0]) + __uint_as_float(s[1]);
}
__device__ __forceinline__ float xrow_sum(float x) {
  x = xrow16_sum(x);
  auto t = __builtin_amdgcn_permlane32_swap(__float_as_uint(x), __float_as_uint(x), false, false);
  return __uint_as_float(t[0]) + __uint_as_float(t[1]);
}
template <int CTRL> __device__ __forceinline__ float dpp(float x) {
  return __builtin_bit_cast(float, __builtin_amdgcn_mov_dpp(__builtin_bit_cast(int, x), CTRL, 0xf, 0xf, true));
}
constexpr int ROR1 = 0x121, ROR2 = 0x122, ROR4 = 0x124, ROR8 = 0x128, QUAD_EVEN = 0xa0  , QUAD_ODD = 0xf5  ;
__device__ __forceinline__ float row_sum(float x) { x += dpp<ROR8>(x); x += dpp<ROR4>(x); x += dpp<ROR2>(x); return x + dpp<ROR1>(x); }
__device__ __forceinline__ float bflo(unsigned u) { return __uint_as_float(u << 16); }
__device__ __forceinline__ float bfhi(unsigned u) { return __uint_as_float(u & 0xffff0000u); }
__device__ __forceinline__ unsigned not_empty(float x) { return min(__float_as_uint(x), EMPTY - 1u); }
template <int N> __device__ __forceinline__ void waitvm() { asm volatile("s_waitcnt vmcnt(%0)" ::"n"(N) : "memory"); }
__device__ __forceinline__ unsigned clock16() { return (unsigned)__builtin_amdgcn_s_memrealtime() * 16u; }

__global__ void __launch_bounds__(NTHR, 2) attn_kernel(const unsigned short* __restrict__ kc, const unsigned short* __restrict__ vc, const unsigned short* __restrict__ q,
                                                       unsigned char* __restrict__ ws, const unsigned short* __restrict__ kt, const unsigned short* __restrict__ vt,
                                                       const unsigned short* __restrict__ mask, unsigned short* __restrict__ out) {
  __shared__ __attribute__((aligned(16))) float lds[L_WORDS];
  const unsigned long long tent = __builtin_amdgcn_s_memrealtime();
  unsigned long long pc0;
  asm volatile("s_getpc_b64 %0" : "=s"(pc0));
  SB();
  const int lane = threadIdx.x & 63, wave = __builtin_amdgcn_readfirstlane(threadIdx.x >> 6);
  const int grp = (blockIdx.x >> 5) * 8 + (blockIdx.x & 7), h = grp >> 2, s = (grp & 3) * GS + ((blockIdx.x >> 3) & 3);
  const unsigned wgoff = (unsigned)(grp * (GS * NSW * TILES) + (s % GS) * NSW) * 4096u;
  const int a = lane >> 4, n = lane & 15;
  unsigned base = wgoff + wave * 4096u;
  unsigned koff = (lane & 7) * 256 + (lane >> 3) * 16;
  asm volatile("" : "+s"(base));
  asm volatile("" : "+v"(koff));
  SB();
  asm volatile("" :: "s"(kc), "s"(vc), "s"(q), "s"(tent));

  if (__builtin_expect(wave == NSW, 0)) {
    unsigned char* const wsh = ws + (size_t)h * WS_HEAD;
    const __amdgpu_buffer_rsrc_t wr = rsrc(wsh, WS_HEAD), orr = rsrc(out, HQ * D * 2);
    GAS unsigned* const zw = (GAS unsigned*)(wsh + WS_Z);
    GAS unsigned* const xw = (GAS unsigned*)(wsh + WS_X);
    const bool leader = s == 4 + 8 * (h & 1) + ((h >> 1) & 3);
    float st = -INFINITY;
    f32x4 vtf = {0.f, 0.f, 0.f, 0.f};
    unsigned x = 0, xs = 0, z = 0;
#define UNTIL(t) while ((unsigned)(__builtin_amdgcn_s_memrealtime() - tent) < (t)) __builtin_amdgcn_s_sleep(16);
#define ECHO() { const unsigned xn = __hip_atomic_load(xw + n * 32, RLX_AGENT); if (lane < NSPLIT && xn != x) __hip_atomic_store(zw + lane, xn, RLX_AGENT); x = xn; }
#define TOUCH_V() { const unsigned t = *(const GAS unsigned*)((const GAS char*)vc + wgoff); asm volatile("" :: "v"(t)); }
    if (!leader) {
      xs = __builtin_amdgcn_readfirstlane(__hip_atomic_load(zw + s, RLX_AGENT)) + 1u;
      if (lane < 32) __hip_atomic_store(xw + s * 32 + lane, xs, RLX_AGENT);
      if ((blockIdx.x >> 3) == 1) {
        unsigned long long p = pc0 & ~0xffull;
        unsigned d;
#pragma unroll 1
        for (int i = 0; i < CODE_BYTES / 512; ++i, p += 512)
          asm volatile("s_load_dword %0, %1, 0\n\ts_load_dword %0, %1, 64\n\ts_load_dword %0, %1, 128\n\ts_load_dword %0, %1, 192\n\t"
                     "s_load_dword %0, %1, 256\n\ts_load_dword %0, %1, 320\n\ts_load_dword %0, %1, 384\n\ts_load_dword %0, %1, 448\n\ts_waitcnt lgkmcnt(0)"
                     : "=&s"(d) : "s"(p));
      }
      UNTIL(1400)
      z = __builtin_amdgcn_readfirstlane(__hip_atomic_load(zw + s, RLX_AGENT));
      TOUCH_V()
      if (z != xs) {
        UNTIL(2700)
        z = __builtin_amdgcn_readfirstlane(__hip_atomic_load(zw + s, RLX_AGENT));
      }
    } else {
      const u32x4 e4 = {EMPTY, EMPTY, EMPTY, EMPTY};
#pragma unroll
      for (int i = 0; i < NSPLIT; ++i) __builtin_amdgcn_raw_buffer_store_b128(e4, wr, WS_SLOT + i * SLOT_BYTES + lane * 16, 0, AUX_SC1);
#pragma unroll
      for (int i = 0; i < 2; ++i) __builtin_amdgcn_raw_buffer_store_b128(e4, wr, WS_SLOT + (8 * i + (lane >> 3)) * SLOT_BYTES + 1024 + (lane & 7) * 16, 0, AUX_SC1);
      const u32x2 qq = *(const GAS u32x2*)((const GAS char*)q + h * (G * D * 2) + lane * 8);
      const u32x2 kk = *(const GAS u32x2*)((const GAS char*)kt + h * (D * 2) + (lane & 31) * 8);
      const unsigned vv = *(const GAS unsigned*)((const GAS char*)vt + h * (D * 2) + lane * 4);
      const unsigned mk = *(const GAS unsigned short*)mask;
      float d = bflo(qq[0]) * bflo(kk[0]);
      d = __builtin_fmaf(bfhi(qq[0]), bfhi(kk[0]), d);
      d = __builtin_fmaf(bflo(qq[1]), bflo(kk[1]), d);
      d = __builtin_fmaf(bfhi(qq[1]), bfhi(kk[1]), d);
      d = xrow16_sum(row_sum(d));
      d = __shfl(d, (n >> 3) * 32);
      st = (d * SCALE + (1.0f - bflo(mk)) * -1000000000.0f) * LOG2E;
      lds[L_VT + lane] = __uint_as_float(vv);
      asm volatile("s_waitcnt lgkmcnt(0)" ::: "memory");
#pragma unroll
      for (int j = 0; j < 4; ++j) vtf[j] = bflo(((const unsigned short*)&lds[L_VT])[32 * a + 8 * j + (n & 7)]);
      waitvm<0>();
      UNTIL(800)
      x = __hip_atomic_load(xw + n * 32, RLX_AGENT);
      if (lane < 32) __hip_atomic_store(zw + lane, x, RLX_AGENT);
      TOUCH_V()
      UNTIL(2100)
      ECHO()
    }
    const bool own = (lane >> 2) == s;
    const unsigned ooff = WS_SLOT + (lane >> 2) * SLOT_BYTES + s * 64 + (lane & 3) * 16, moff = WS_SLOT + (lane >> 2) * SLOT_BYTES + 1024 + ((s >> 1) & 1) * 8;
    const unsigned poff = WS_SLOT + s * SLOT_BYTES + lane * 16, pmoff = (lane & 55) == 0 ? WS_SLOT + s * SLOT_BYTES + 1024 + lane : OOB;
    const unsigned outoff = (lane & 12) == 0 ? (h * (G * D) + ((s >> 1) & 1) * D + 32 * (s >> 2) + 8 * a + 4 * (s & 1) + (lane & 3)) * 2 : OOB;
    const float lt = a == 0 ? 1.0f : 0.0f;
    SB();
    __builtin_amdgcn_s_barrier();
    asm volatile("" ::: "memory");
    unsigned spins = 0;
#pragma unroll 1
    while (__builtin_expect(z != xs, 0)) {
      if (++spins > SPIN_CAP) return;
      __builtin_amdgcn_s_sleep(32);
      z = __builtin_amdgcn_readfirstlane(__hip_atomic_load(zw + s, RLX_AGENT));
    }
    f32x2 mlw[NSW];
    f32x4 ow[NSW];
#pragma unroll
    for (int w = 0; w < NSW; ++w) mlw[w] = *(const f32x2*)&lds[L_ML + w * 128 + 2 * lane];
#pragma unroll
    for (int w = 0; w < NSW; ++w) ow[w] = *(const f32x4*)&lds[L_O + w * 256 + 4 * lane];
    SB();
    u32x4 o = __builtin_amdgcn_raw_buffer_load_b128(wr, ooff, 0, AUX_SC1);
    u32x2 ml = __builtin_amdgcn_raw_buffer_load_b64(wr, moff, 0, AUX_SC1);
    const unsigned touch = __builtin_amdgcn_raw_buffer_load_b32(orr, lane == 0 ? outoff : OOB, 0, 0);
    SB();
    float M = st;
#pragma unroll
    for (int w = 0; w < NSW; ++w) M = vmax(M, mlw[w][0]);
    const float wt0 = __builtin_amdgcn_exp2f(st - M);
    float L = wt0 * lt;
    f32x4 acc = vtf * wt0;
#pragma unroll
    for (int w = 0; w < NSW; ++w) { const float wt = __builtin_amdgcn_exp2f(mlw[w][0] - M); L = __builtin_fmaf(wt, mlw[w][1], L); acc += ow[w] * wt; }
    const u32x4 po = {not_empty(acc[0]), not_empty(acc[1]), not_empty(acc[2]), not_empty(acc[3])};
    __builtin_amdgcn_raw_buffer_store_b128(po, wr, poff, 0, AUX_SC1);
    SB();
    const u32x2 pm = {not_empty(M), not_empty(xrow_sum(L))};
    __builtin_amdgcn_raw_buffer_store_b64(pm, wr, pmoff, 0, AUX_SC1);
    SB();
    asm volatile("" : "+v"(o), "+v"(ml));
    if (own) { o = po; ml = pm; }
#define PENDING() (max(max(max(o[0], o[1]), max(o[2], o[3])), max(ml[0], ml[1])) == EMPTY)
    bool pend = PENDING();
    unsigned long long any = __builtin_amdgcn_ballot_w64(pend);
    asm volatile("" : "+s"(any));
#pragma unroll 1
    while (any != 0ull && ++spins <= SPIN_CAP) {
      if (leader && (spins & 7u) == 4u) ECHO()
      asm volatile("" ::: "memory");
      const u32x4 on = __builtin_amdgcn_raw_buffer_load_b128(wr, pend ? ooff : OOB, 0, AUX_SC1);
      const u32x2 mn = __builtin_amdgcn_raw_buffer_load_b64(wr, pend ? moff : OOB, 0, AUX_SC1);
      if (pend) { o = on; ml = mn; }
      pend = PENDING();
      any = __builtin_amdgcn_ballot_w64(pend);
    }
    const float ms = __uint_as_float(ml[0]);
    float Mf = vmax(ms, dpp<ROR4>(ms));
    Mf = xrow_max(vmax(Mf, dpp<ROR8>(Mf)));
    const float wf = __builtin_amdgcn_exp2f(ms - Mf);
    float t[5];
    t[4] = wf * __uint_as_float(ml[1]);
#pragma unroll
    for (int k = 0; k < 4; ++k) t[k] = wf * __uint_as_float(o[k]);
    asm volatile("" : "+v"(t[0]), "+v"(t[1]), "+v"(t[2]), "+v"(t[3]), "+v"(t[4]));
#pragma unroll
    for (int k = 4; k >= 0; --k) t[k] += dpp<ROR4>(t[k]);
    asm volatile("" : "+v"(t[0]), "+v"(t[1]), "+v"(t[2]), "+v"(t[3]), "+v"(t[4]));
#pragma unroll
    for (int k = 4; k >= 0; --k) t[k] += dpp<ROR8>(t[k]);
    asm volatile("" : "+v"(t[0]), "+v"(t[1]), "+v"(t[2]), "+v"(t[3]), "+v"(t[4]));
    const float inv = __builtin_amdgcn_rcpf(xrow_sum(t[4]));
    const auto s01 = __builtin_amdgcn_permlane16_swap(__float_as_uint(t[0]), __float_as_uint(t[1]), false, false);
    const auto s23 = __builtin_amdgcn_permlane16_swap(__float_as_uint(t[2]), __float_as_uint(t[3]), false, false);
    const float u = __uint_as_float(s01[0]) + __uint_as_float(s01[1]), v = __uint_as_float(s23[0]) + __uint_as_float(s23[1]);
    const auto suv = __builtin_amdgcn_permlane32_swap(__float_as_uint(u), __float_as_uint(v), false, false);
    const float r = (__uint_as_float(suv[0]) + __uint_as_float(suv[1])) * inv;
    __builtin_amdgcn_raw_buffer_store_b16((unsigned short)cvtpk(r, r), orr, outoff, 0, AUX_SC1);
    asm volatile("" :: "v"(touch));
    return;
  }

  const unsigned voff = a * 1024 + n * 16;
  u32x4 Q[2], R0[4], R1[4], R2[4], R3[4];
  const __amdgpu_buffer_rsrc_t kr = rsrc(kc, KV_BYTES);
#define LDK(R, o) { _Pragma("unroll") for (int j = 0; j < 4; ++j) R[j] = __builtin_amdgcn_raw_buffer_load_b128(kr, koff + (j & 1) * 128 + (j >> 1) * 2048, (o), AUX_NT); SB(); }
#define LDV(R, o, nb) { const __amdgpu_buffer_rsrc_t vr = rsrc(vc, (nb)); \
    _Pragma("unroll") for (int j = 0; j < 4; ++j) R[j] = __builtin_amdgcn_raw_buffer_load_b128(vr, voff + j * 256, (o), AUX_NT); SB(); }
  LDK(R0, base)
  { const __amdgpu_buffer_rsrc_t qr = rsrc(q, HQ * D * 2);
#pragma unroll
    for (int j = 0; j < 2; ++j) Q[j] = __builtin_amdgcn_raw_buffer_load_b128(qr, (n >> 3) * 256 + (2 * a + (n & 1)) * 16 + j * 128, h * (G * D * 2), 0);
    SB(); }
  LDK(R1, base + TSTEP)
  float* const sw = &lds[L_S + wave * (TILES * 32) + a * 8 + (n >> 3) * 4];
  float mx = -INFINITY;
  unsigned so = base + 2 * TSTEP;
  waitvm<4>();
  asm volatile("" : "+v"(Q[0]), "+v"(Q[1]));
  SB();
  unsigned turn = clock16();
  { const unsigned cap = (unsigned)tent * 16u + T0CAP;
    if ((int)(turn - cap) > 0) turn = cap;
    const unsigned ph = (((blockIdx.x >> 5) + blockIdx.x) & 7) * (PACE / 8);
    turn += PACE / 2 - ((turn & 0x0fffffffu) + PACE / 2 + PACE - ph) % PACE; }
#define TURN() { while ((int)(turn - clock16()) > 0) {} turn += PACE; SB(); }
#define QK(R, t) { \
    const f32x4 z4 = {0.f, 0.f, 0.f, 0.f}; \
    const f32x4 d0 = mfma16(R[1], Q[1], mfma16(R[0], Q[0], z4)), d1 = mfma16(R[3], Q[1], mfma16(R[2], Q[0], z4)); \
    f32x4 S; \
    _Pragma("unroll") for (int j = 0; j < 4; ++j) { const auto x = __builtin_amdgcn_permlane32_swap(__float_as_uint(d0[j]), __float_as_uint(d1[j]), false, false); \
      S[j] = __uint_as_float(x[0]) + dpp<QUAD_ODD>(__uint_as_float(x[1])); }   \
    mx = vmax(vmax(vmax(mx, S[0]), S[1]), vmax(S[2], S[3])); \
    if ((n & 7) == 0) *(f32x4*)&sw[(t) * 32] = S; \
    asm volatile("" : "+v"(mx)); SB(); }
#pragma unroll 1
  for (int t = 0; t < TILES - 4; t += 4) {
    TURN() LDK(R2, so) QK(R0, t)
    TURN() LDK(R3, so + TSTEP) QK(R1, t + 1)
    TURN() LDK(R0, so + 2 * TSTEP) QK(R2, t + 2)
    TURN() LDK(R1, so + 3 * TSTEP) QK(R3, t + 3)
    so += 4 * TSTEP;
  }
  TURN() LDK(R2, so) QK(R0, TILES - 4)
  TURN() LDK(R3, so + TSTEP) QK(R1, TILES - 3)
  TURN() LDV(R0, base, KV_BYTES) QK(R2, TILES - 2)
  TURN() LDV(R1, base + TSTEP, KV_BYTES) QK(R3, TILES - 1)
  const float mc = xrow_max(dpp<QUAD_EVEN>(mx)) * C2;
  unsigned msk[4];
#pragma unroll
  for (int w = 0; w < 4; ++w) msk[w] = ((n & 7) >> 1) == w ? ((n & 1) ? 0xffff0000u : 0x0000ffffu) : 0u;
  float l = 0.f;
  f32x4 O = {0.f, 0.f, 0.f, 0.f};
#define PV(R, RN, o, nb, t) { \
    const f32x4 S = *(const f32x4*)&sw[(t) * 32]; \
    float p[4]; u32x4 B[4]; \
    _Pragma("unroll") for (int j = 0; j < 4; ++j) p[j] = __builtin_amdgcn_exp2f(__builtin_fmaf(S[j], C2, -mc)); \
    l += (p[0] + p[1]) + (p[2] + p[3]); \
    _Pragma("unroll") for (int j = 0; j < 4; ++j) { const unsigned pp = cvtpk(p[j], p[j]); B[j] = u32x4{pp & msk[0], pp & msk[1], pp & msk[2], pp & msk[3]}; } \
    asm volatile("" : "+v"(B[0]), "+v"(B[1]), "+v"(B[2]), "+v"(B[3]), "+v"(l)); \
    SB(); \
    TURN() LDV(RN, o, nb) \
    _Pragma("unroll") for (int j = 0; j < 4; ++j) O = mfma16(R[j], B[j], O); \
    asm volatile("" : "+v"(O)); \
    SB(); }
  so = base + 2 * TSTEP;
#pragma unroll 1
  for (int t = 0; t < TILES; t += 4) {
    const bool more = t < TILES - 4;
    const unsigned nb = more ? KV_BYTES : 0u, so2 = more ? so : 0u;
    PV(R0, R2, so, KV_BYTES, t)
    PV(R1, R3, so + TSTEP, KV_BYTES, t + 1)
    if (!more) turn -= 1u << 24;
    PV(R2, R0, so2 + 2 * TSTEP, nb, t + 2)
    PV(R3, R1, so2 + 3 * TSTEP, nb, t + 3)
    so += 4 * TSTEP;
  }
  *(f32x2*)&lds[L_ML + wave * 128 + 2 * lane] = f32x2{mc, l};
  *(f32x4*)&lds[L_O + wave * 256 + 4 * lane] = O;
  asm volatile("s_waitcnt lgkmcnt(0)" ::: "memory");
  __builtin_amdgcn_s_barrier();
}
}

extern "C" void kernel_launch(void* const* d_in, const int* in_sizes, int n_in, void* d_out, int out_size, void* d_ws, size_t ws_size, hipStream_t stream) {
  const unsigned short* q = (const unsigned short*)d_in[0];
  const unsigned short* kc = (const unsigned short*)d_in[1];
  const unsigned short* vc = (const unsigned short*)d_in[2];
  const unsigned short* kt = (const unsigned short*)d_in[3];
  const unsigned short* vt = (const unsigned short*)d_in[4];
  const unsigned short* mask = (const unsigned short*)d_in[5];
  attn_kernel<<<HKV * NSPLIT, NTHR, 0, stream>>>(kc, vc, q, (unsigned char*)d_ws, kt, vt, mask, (unsigned short*)d_out);
}
```

```cpp
#include <hip/hip_runtime.h>
#include <hip/hip_bf16.h>
#include <stdint.h>

namespace {
constexpr int HQ = 32, HKV = 16, CTX = 32768, D = 128, G = HQ / HKV;
constexpr int NSPLIT = 16, NSW = 4;
constexpr int NTHR = 64 * (NSW + 1);
constexpr int TILES = CTX / 16 / NSPLIT / NSW;
constexpr int GS = 4;
constexpr unsigned KV_BYTES = (unsigned)HKV * CTX * D * 2;
constexpr unsigned TSTEP = GS * NSW * 4096;
constexpr float SCALE = 0.088388347648318440f, LOG2E = 1.4426950408889634f, C2 = SCALE * LOG2E;
constexpr unsigned PACE = 857, T0CAP = 90 * 16;
constexpr unsigned WS_X = 0, WS_Z = 2048, WS_SLOT = 4096, SLOT_BYTES = 1024 + 128, WS_HEAD = 32768;
constexpr unsigned EMPTY = 0xffffffffu;
constexpr unsigned OOB = 0x40000000u;
constexpr unsigned SPIN_CAP = 1u << 18;
constexpr int CODE_BYTES = 6144;
constexpr int L_O = 0, L_ML = NSW * 256, L_VT = L_ML + NSW * 128, L_S = L_VT + 64, L_WORDS = L_S + NSW * TILES * 32;

typedef unsigned u32x2 __attribute__((ext_vector_type(2)));
typedef unsigned u32x4 __attribute__((ext_vector_type(4)));
typedef float f32x2 __attribute__((ext_vector_type(2)));
typedef float f32x4 __attribute__((ext_vector_type(4)));
typedef __bf16 bf16x2_t __attribute__((ext_vector_type(2)));
typedef __bf16 bf16x8_t __attribute__((ext_vector_type(8)));
#define GAS __attribute__((address_space(1)))
#define RLX_AGENT __ATOMIC_RELAXED, __HIP_MEMORY_SCOPE_AGENT
#define SB() __builtin_amdgcn_sched_barrier(0)
constexpr int AUX_NT = 2, AUX_SC1 = 16, RSRC_W3 = 0x00020000;

__device__ __forceinline__ __amdgpu_buffer_rsrc_t rsrc(const void* p, unsigned bytes) { return __builtin_amdgcn_make_buffer_rsrc((void*)p, 0, bytes, RSRC_W3); }
__device__ __forceinline__ f32x4 mfma16(u32x4 a, u32x4 b, f32x4 c) {
  return __builtin_amdgcn_mfma_f32_16x16x32_bf16(__builtin_bit_cast(bf16x8_t, a), __builtin_bit_cast(bf16x8_t, b), c, 0, 0, 0);
}
__device__ __forceinline__ unsigned cvtpk(float lo, float hi) {
  f32x2 v = {lo, hi};
  return __builtin_bit_cast(unsigned, __builtin_convertvector(v, bf16x2_t));
}
__device__ __forceinline__ float vmax(float a, float b) { return __builtin_elementwise_maximum(a, b); }
__device__ __forceinline__ float xrow_max(float x) {
  auto s = __builtin_amdgcn_permlane16_swap(__float_as_uint(x), __float_as_uint(x), false, false);
  x = vmax(__uint_as_float(s[0]), __uint_as_float(s[1]));
  auto t = __builtin_amdgcn_permlane32_swap(__float_as_uint(x), __float_as_uint(x), false, false);
  return vmax(__uint_as_float(t[0]), __uint_as_float(t[1]));
}
__device__ __forceinline__ float xrow16_sum(float x) {
  auto s = __builtin_amdgcn_permlane16_swap(__float_as_uint(x), __float_as_uint(x), false, false);
  return __uint_as_float(s[0]) + __uint_as_float(s[1]);
}
__device__ __forceinline__ float xrow_sum(float x) {
  x = xrow16_sum(x);
  auto t = __builtin_amdgcn_permlane32_swap(__float_as_uint(x), __float_as_uint(x), false, false);
  return __uint_as_float(t[0]) + __uint_as_float(t[1]);
}
template <int CTRL> __device__ __forceinline__ float dpp(float x) {
  return __builtin_bit_cast(float, __builtin_amdgcn_mov_dpp(__builtin_bit_cast(int, x), CTRL, 0xf, 0xf, true));
}
constexpr int ROR1 = 0x121, ROR2 = 0x122, ROR4 = 0x124, ROR8 = 0x128, QUAD_EVEN = 0xa0  , QUAD_ODD = 0xf5  ;
__device__ __forceinline__ float row_sum(float x) { x += dpp<ROR8>(x); x += dpp<ROR4>(x); x += dpp<ROR2>(x); return x + dpp<ROR1>(x); }
__device__ __forceinline__ float bflo(unsigned u) { return __uint_as_float(u << 16); }
__device__ __forceinline__ float bfhi(unsigned u) { return __uint_as_float(u & 0xffff0000u); }
__device__ __forceinline__ unsigned not_empty(float x) { return min(__float_as_uint(x), EMPTY - 1u); }
template <int N> __device__ __forceinline__ void waitvm() { asm volatile("s_waitcnt vmcnt(%0)" ::"n"(N) : "memory"); }
__device__ __forceinline__ unsigned clock16() { return (unsigned)__builtin_amdgcn_s_memrealtime() * 16u; }

__global__ void __launch_bounds__(NTHR, 2) attn_kernel(const unsigned short* __restrict__ kc, const unsigned short* __restrict__ vc, const unsigned short* __restrict__ q,
                                                       unsigned char* __restrict__ ws, const unsigned short* __restrict__ kt, const unsigned short* __restrict__ vt,
                                                       const unsigned short* __restrict__ mask, unsigned short* __restrict__ out) {
  __shared__ __attribute__((aligned(16))) float lds[L_WORDS];
  const unsigned long long tent = __builtin_amdgcn_s_memrealtime();
  unsigned long long pc0;
  asm volatile("s_getpc_b64 %0" : "=s"(pc0));
  SB();
  const int lane = threadIdx.x & 63, wave = __builtin_amdgcn_readfirstlane(threadIdx.x >> 6);
  const int grp = (blockIdx.x >> 5) * 8 + (blockIdx.x & 7), h = grp >> 2, s = (grp & 3) * GS + ((blockIdx.x >> 3) & 3);
  const unsigned wgoff = (unsigned)(grp * (GS * NSW * TILES) + (s % GS) * NSW) * 4096u;
  const int a = lane >> 4, n = lane & 15;
  unsigned base = wgoff + wave * 4096u;
  unsigned koff = (lane & 7) * 256 + (lane >> 3) * 16;
  asm volatile("" : "+s"(base));
  asm volatile("" : "+v"(koff));
  SB();
  asm volatile("" :: "s"(kc), "s"(vc), "s"(q), "s"(tent));

  if (__builtin_expect(wave == NSW, 0)) {
    unsigned char* const wsh = ws + (size_t)h * WS_HEAD;
    const __amdgpu_buffer_rsrc_t wr = rsrc(wsh, WS_HEAD), orr = rsrc(out, HQ * D * 2);
    GAS unsigned* const zw = (GAS unsigned*)(wsh + WS_Z);
    GAS unsigned* const xw = (GAS unsigned*)(wsh + WS_X);
    const bool leader = s == 4 + 8 * (h & 1) + ((h >> 1) & 3);
    float st = -INFINITY;
    f32x4 vtf = {0.f, 0.f, 0.f, 0.f};
    unsigned x = 0, xs = 0, z = 0;
#define UNTIL(t) while ((unsigned)(__builtin_amdgcn_s_memrealtime() - tent) < (t)) __builtin_amdgcn_s_sleep(16);
#define ECHO() { const unsigned xn = __hip_atomic_load(xw + n * 32, RLX_AGENT); if (lane < NSPLIT && xn != x) __hip_atomic_store(zw + lane, xn, RLX_AGENT); x = xn; }
#define TOUCH_V() { const unsigned t = *(const GAS unsigned*)((const GAS char*)vc + wgoff); asm volatile("" :: "v"(t)); }
    if (!leader) {
      xs = __builtin_amdgcn_readfirstlane(__hip_atomic_load(zw + s, RLX_AGENT)) + 1u;
      if (lane < 32) __hip_atomic_store(xw + s * 32 + lane, xs, RLX_AGENT);
      if ((blockIdx.x >> 3) == 1 || (blockIdx.x >> 3) == 2) {
        unsigned long long p = pc0 & ~0xffull;
        unsigned d;
#pragma unroll 1
        for (int i = 0; i < CODE_BYTES / 512; ++i, p += 512)
          asm volatile("s_load_dword %0, %1, 0\n\ts_load_dword %0, %1, 64\n\ts_load_dword %0, %1, 128\n\ts_load_dword %0, %1, 192\n\t"
                     "s_load_dword %0, %1, 256\n\ts_load_dword %0, %1, 320\n\ts_load_dword %0, %1, 384\n\ts_load_dword %0, %1, 448\n\ts_waitcnt lgkmcnt(0)"
                     : "=&s"(d) : "s"(p));
      }
      UNTIL(1400)
      z = __builtin_amdgcn_readfirstlane(__hip_atomic_load(zw + s, RLX_AGENT));
      TOUCH_V()
      if (z != xs) {
        UNTIL(2700)
        z = __builtin_amdgcn_readfirstlane(__hip_atomic_load(zw + s, RLX_AGENT));
      }
    } else {
      const u32x4 e4 = {EMPTY, EMPTY, EMPTY, EMPTY};
#pragma unroll
      for (int i = 0; i < NSPLIT; ++i) __builtin_amdgcn_raw_buffer_store_b128(e4, wr, WS_SLOT + i * SLOT_BYTES + lane * 16, 0, AUX_SC1);
#pragma unroll
      for (int i = 0; i < 2; ++i) __builtin_amdgcn_raw_buffer_store_b128(e4, wr, WS_SLOT + (8 * i + (lane >> 3)) * SLOT_BYTES + 1024 + (lane & 7) * 16, 0, AUX_SC1);
      const u32x2 qq = *(const GAS u32x2*)((const GAS char*)q + h * (G * D * 2) + lane * 8);
      const u32x2 kk = *(const GAS u32x2*)((const GAS char*)kt + h * (D * 2) + (lane & 31) * 8);
      const unsigned vv = *(const GAS unsigned*)((const GAS char*)vt + h * (D * 2) + lane * 4);
      const unsigned mk = *(const GAS unsigned short*)mask;
      float d = bflo(qq[0]) * bflo(kk[0]);
      d = __builtin_fmaf(bfhi(qq[0]), bfhi(kk[0]), d);
      d = __builtin_fmaf(bflo(qq[1]), bflo(kk[1]), d);
      d = __builtin_fmaf(bfhi(qq[1]), bfhi(kk[1]), d);
      d = xrow16_sum(row_sum(d));
      d = __shfl(d, (n >> 3) * 32);
      st = (d * SCALE + (1.0f - bflo(mk)) * -1000000000.0f) * LOG2E;
      lds[L_VT + lane] = __uint_as_float(vv);
      asm volatile("s_waitcnt lgkmcnt(0)" ::: "memory");
#pragma unroll
      for (int j = 0; j < 4; ++j) vtf[j] = bflo(((const unsigned short*)&lds[L_VT])[32 * a + 8 * j + (n & 7)]);
      waitvm<0>();
      UNTIL(800)
      x = __hip_atomic_load(xw + n * 32, RLX_AGENT);
      if (lane < 32) __hip_atomic_store(zw + lane, x, RLX_AGENT);
      TOUCH_V()
      UNTIL(2100)
      ECHO()
    }
    const bool own = (lane >> 2) == s;
    unsigned ooff = WS_SLOT + (lane >> 2) * SLOT_BYTES + s * 64 + (lane & 3) * 16, moff = WS_SLOT + (lane >> 2) * SLOT_BYTES + 1024 + ((s >> 1) & 1) * 8;
    unsigned poff = WS_SLOT + s * SLOT_BYTES + lane * 16, pmoff = (lane & 55) == 0 ? WS_SLOT + s * SLOT_BYTES + 1024 + lane : OOB;
    unsigned outoff = (lane & 12) == 0 ? (h * (G * D) + ((s >> 1) & 1) * D + 32 * (s >> 2) + 8 * a + 4 * (s & 1) + (lane & 3)) * 2 : OOB;
    float lt = a == 0 ? 1.0f : 0.0f;
    unsigned lofs = lane * 8;
    asm volatile("" : "+v"(ooff), "+v"(moff), "+v"(poff), "+v"(pmoff), "+v"(outoff), "+v"(lt), "+v"(lofs));
    __builtin_amdgcn_s_barrier();
    asm volatile("" ::: "memory");
    unsigned spins = 0;
#pragma unroll 1
    while (__builtin_expect(z != xs, 0)) {
      if (++spins > SPIN_CAP) return;
      __builtin_amdgcn_s_sleep(32);
      z = __builtin_amdgcn_readfirstlane(__hip_atomic_load(zw + s, RLX_AGENT));
    }
    f32x2 mlw[NSW];
    f32x4 ow[NSW];
#pragma unroll
    for (int w = 0; w < NSW; ++w) mlw[w] = *(const f32x2*)((const char*)&lds[L_ML + w * 128] + lofs);
#pragma unroll
    for (int w = 0; w < NSW; ++w) ow[w] = *(const f32x4*)((const char*)&lds[L_O + w * 256] + 2 * lofs);
    SB();
    float M = st;
#pragma unroll
    for (int w = 0; w < NSW; ++w) M = vmax(M, mlw[w][0]);
    const float wt0 = __builtin_amdgcn_exp2f(st - M);
    float L = wt0 * lt;
    f32x4 acc = vtf * wt0;
#pragma unroll
    for (int w = 0; w < NSW; ++w) { const float wt = __builtin_amdgcn_exp2f(mlw[w][0] - M); L = __builtin_fmaf(wt, mlw[w][1], L); acc += ow[w] * wt; }
    const u32x4 po = {not_empty(acc[0]), not_empty(acc[1]), not_empty(acc[2]), not_empty(acc[3])};
    __builtin_amdgcn_raw_buffer_store_b128(po, wr, poff, 0, AUX_SC1);
    SB();
    const u32x2 pm = {not_empty(M), not_empty(xrow_sum(L))};
    __builtin_amdgcn_raw_buffer_store_b64(pm, wr, pmoff, 0, AUX_SC1);
    SB();
    u32x4 o = po;
    u32x2 ml = pm;
    bool pend = !own;
    unsigned long long any = 1ull;
    __builtin_amdgcn_s_sleep(10);
#pragma unroll 1
    while (any != 0ull && ++spins <= SPIN_CAP) {
      if (leader && (spins & 7u) == 4u) ECHO()
      asm volatile("" ::: "memory");
      const u32x4 on = __builtin_amdgcn_raw_buffer_load_b128(wr, pend ? ooff : OOB, 0, AUX_SC1);
      const u32x2 mn = __builtin_amdgcn_raw_buffer_load_b64(wr, pend ? moff : OOB, 0, AUX_SC1);
      if (pend) { o = on; ml = mn; }
      pend = max(max(max(o[0], o[1]), max(o[2], o[3])), max(ml[0], ml[1])) == EMPTY;
      any = __builtin_amdgcn_ballot_w64(pend);
    }
    const float ms = __uint_as_float(ml[0]);
    float Mf = vmax(ms, dpp<ROR4>(ms));
    Mf = xrow_max(vmax(Mf, dpp<ROR8>(Mf)));
    const float wf = __builtin_amdgcn_exp2f(ms - Mf);
    float t[5];
    t[4] = wf * __uint_as_float(ml[1]);
#pragma unroll
    for (int k = 0; k < 4; ++k) t[k] = wf * __uint_as_float(o[k]);
    asm volatile("" : "+v"(t[0]), "+v"(t[1]), "+v"(t[2]), "+v"(t[3]), "+v"(t[4]));
#pragma unroll
    for (int k = 4; k >= 0; --k) t[k] += dpp<ROR4>(t[k]);
    asm volatile("" : "+v"(t[0]), "+v"(t[1]), "+v"(t[2]), "+v"(t[3]), "+v"(t[4]));
#pragma unroll
    for (int k = 4; k >= 0; --k) t[k] += dpp<ROR8>(t[k]);
    asm volatile("" : "+v"(t[0]), "+v"(t[1]), "+v"(t[2]), "+v"(t[3]), "+v"(t[4]));
    const float inv = __builtin_amdgcn_rcpf(xrow_sum(t[4]));
    const auto s01 = __builtin_amdgcn_permlane16_swap(__float_as_uint(t[0]), __float_as_uint(t[1]), false, false);
    const auto s23 = __builtin_amdgcn_permlane16_swap(__float_as_uint(t[2]), __float_as_uint(t[3]), false, false);
    const float u = __uint_as_float(s01[0]) + __uint_as_float(s01[1]), v = __uint_as_float(s23[0]) + __uint_as_float(s23[1]);
    const auto suv = __builtin_amdgcn_permlane32_swap(__float_as_uint(u), __float_as_uint(v), false, false);
    const float r = (__uint_as_float(suv[0]) + __uint_as_float(suv[1])) * inv;
    __builtin_amdgcn_raw_buffer_store_b16((unsigned short)cvtpk(r, r), orr, outoff, 0, AUX_SC1);
    return;
  }

  const unsigned voff = a * 1024 + n * 16;
  u32x4 Q[2], R0[4], R1[4], R2[4], R3[4];
  const __amdgpu_buffer_rsrc_t kr = rsrc(kc, KV_BYTES);
#define LDK(R, o) { _Pragma("unroll") for (int j = 0; j < 4; ++j) R[j] = __builtin_amdgcn_raw_buffer_load_b128(kr, koff + (j & 1) * 128 + (j >> 1) * 2048, (o), AUX_NT); SB(); }
#define LDV(R, o, nb) { const __amdgpu_buffer_rsrc_t vr = rsrc(vc, (nb)); \
    _Pragma("unroll") for (int j = 0; j < 4; ++j) R[j] = __builtin_amdgcn_raw_buffer_load_b128(vr, voff + j * 256, (o), AUX_NT); SB(); }
  LDK(R0, base)
  { const __amdgpu_buffer_rsrc_t qr = rsrc(q, HQ * D * 2);
#pragma unroll
    for (int j = 0; j < 2; ++j) Q[j] = __builtin_amdgcn_raw_buffer_load_b128(qr, (n >> 3) * 256 + (2 * a + (n & 1)) * 16 + j * 128, h * (G * D * 2), 0);
    SB(); }
  LDK(R1, base + TSTEP)
  float* const sw = &lds[L_S + wave * (TILES * 32) + a * 8 + (n >> 3) * 4];
  float mx = -INFINITY;
  unsigned so = base + 2 * TSTEP;
  const unsigned cap = clock16() + T0CAP;
  SB();
  waitvm<4>();
  asm volatile("" : "+v"(Q[0]), "+v"(Q[1]));
  SB();
  unsigned turn = clock16();
  if ((int)(turn - cap) > 0) turn = cap;
  turn += PACE - ((turn & 0x0fffffffu) + PACE - (((blockIdx.x >> 5) + blockIdx.x) & 7) * (PACE / 8)) % PACE;
#define TURN() { while ((int)(turn - clock16()) > 0) {} turn += PACE; SB(); }
#define QK(R, t) { \
    const f32x4 z4 = {0.f, 0.f, 0.f, 0.f}; \
    const f32x4 d0 = mfma16(R[1], Q[1], mfma16(R[0], Q[0], z4)), d1 = mfma16(R[3], Q[1], mfma16(R[2], Q[0], z4)); \
    f32x4 S; \
    _Pragma("unroll") for (int j = 0; j < 4; ++j) { const auto x = __builtin_amdgcn_permlane32_swap(__float_as_uint(d0[j]), __float_as_uint(d1[j]), false, false); \
      S[j] = __uint_as_float(x[0]) + dpp<QUAD_ODD>(__uint_as_float(x[1])); }   \
    mx = vmax(vmax(vmax(mx, S[0]), S[1]), vmax(S[2], S[3])); \
    if ((n & 7) == 0) *(f32x4*)&sw[(t) * 32] = S; \
    asm volatile("" : "+v"(mx)); SB(); }
#pragma unroll 1
  for (int t = 0; t < TILES - 4; t += 4) {
    TURN() LDK(R2, so) QK(R0, t)
    TURN() LDK(R3, so + TSTEP) QK(R1, t + 1)
    TURN() LDK(R0, so + 2 * TSTEP) QK(R2, t + 2)
    TURN() LDK(R1, so + 3 * TSTEP) QK(R3, t + 3)
    so += 4 * TSTEP;
  }
  TURN() LDK(R2, so) QK(R0, TILES - 4)
  TURN() LDK(R3, so + TSTEP) QK(R1, TILES - 3)
  TURN() LDV(R0, base, KV_BYTES) QK(R2, TILES - 2)
  TURN() LDV(R1, base + TSTEP, KV_BYTES) QK(R3, TILES - 1)
  const float mc = xrow_max(dpp<QUAD_EVEN>(mx)) * C2;
  unsigned msk[4];
#pragma unroll
  for (int w = 0; w < 4; ++w) msk[w] = ((n & 7) >> 1) == w ? ((n & 1) ? 0xffff0000u : 0x0000ffffu) : 0u;
  float l = 0.f;
  f32x4 O = {0.f, 0.f, 0.f, 0.f};
#define PV(R, RN, o, nb, t) { \
    const f32x4 S = *(const f32x4*)&sw[(t) * 32]; \
    float p[4]; u32x4 B[4]; \
    _Pragma("unroll") for (int j = 0; j < 4; ++j) p[j] = __builtin_amdgcn_exp2f(__builtin_fmaf(S[j], C2, -mc)); \
    l += (p[0] + p[1]) + (p[2] + p[3]); \
    _Pragma("unroll") for (int j = 0; j < 4; ++j) { const unsigned pp = cvtpk(p[j], p[j]); B[j] = u32x4{pp & msk[0], pp & msk[1], pp & msk[2], pp & msk[3]}; } \
    asm volatile("" : "+v"(B[0]), "+v"(B[1]), "+v"(B[2]), "+v"(B[3]), "+v"(l)); \
    SB(); \
    TURN() LDV(RN, o, nb) \
    _Pragma("unroll") for (int j = 0; j < 4; ++j) O = mfma16(R[j], B[j], O); \
    asm volatile("" : "+v"(O)); \
    SB(); }
  so = base + 2 * TSTEP;
#pragma unroll 1
  for (int t = 0; t < TILES; t += 4) {
    const bool more = t < TILES - 4;
    const unsigned nb = more ? KV_BYTES : 0u, so2 = more ? so : 0u;
    PV(R0, R2, so, KV_BYTES, t)
    PV(R1, R3, so + TSTEP, KV_BYTES, t + 1)
    if (!more) turn -= 1u << 24;
    PV(R2, R0, so2 + 2 * TSTEP, nb, t + 2)
    PV(R3, R1, so2 + 3 * TSTEP, nb, t + 3)
    so += 4 * TSTEP;
  }
  *(f32x2*)&lds[L_ML + wave * 128 + 2 * lane] = f32x2{mc, l};
  *(f32x4*)&lds[L_O + wave * 256 + 4 * lane] = O;
  asm volatile("s_waitcnt lgkmcnt(0)" ::: "memory");
  __builtin_amdgcn_s_barrier();
}
}

extern "C" void kernel_launch(void* const* d_in, const int* in_sizes, int n_in, void* d_out, int out_size, void* d_ws, size_t ws_size, hipStream_t stream) {
  const unsigned short* q = (const unsigned short*)d_in[0];
  const unsigned short* kc = (const unsigned short*)d_in[1];
  const unsigned short* vc = (const unsigned short*)d_in[2];
  const unsigned short* kt = (const unsigned short*)d_in[3];
  const unsigned short* vt = (const unsigned short*)d_in[4];
  const unsigned short* mask = (const unsigned short*)d_in[5];
  attn_kernel<<<HKV * NSPLIT, NTHR, 0, stream>>>(kc, vc, q, (unsigned char*)d_ws, kt, vt, mask, (unsigned short*)d_out);
}
```

```cpp
#include <hip/hip_runtime.h>
#include <hip/hip_bf16.h>
#include <stdint.h>

namespace {
constexpr int HQ = 32, HKV = 16, CTX = 32768, D = 128, G = HQ / HKV;
constexpr int NSPLIT = 16, NSW = 4;
constexpr int NTHR = 64 * (NSW + 1);
constexpr int TILES = CTX / 16 / NSPLIT / NSW;
constexpr int GS = 4;
constexpr unsigned KV_BYTES = (unsigned)HKV * CTX * D * 2;
constexpr unsigned TSTEP = GS * NSW * 4096;
constexpr float SCALE = 0.088388347648318440f, LOG2E = 1.4426950408889634f, C2 = SCALE * LOG2E;
constexpr unsigned PACE = 857, CAP_ISSUED = 90 * 16, CAP_ARGS = 180 * 16;
constexpr unsigned WS_X = 0, WS_Z = 2048, WS_SLOT = 4096, SLOT_BYTES = 1024 + 128, WS_HEAD = 32768;
constexpr unsigned EMPTY = 0xffffffffu;
constexpr unsigned OOB = 0x40000000u;
constexpr unsigned SPIN_CAP = 1u << 20;
constexpr int CODE_BYTES = 6144;
constexpr int L_O = 0, L_ML = NSW * 256, L_VT = L_ML + NSW * 128, L_S = L_VT + 64, L_WORDS = L_S + NSW * TILES * 32;

typedef unsigned u32x2 __attribute__((ext_vector_type(2)));
typedef unsigned u32x4 __attribute__((ext_vector_type(4)));
typedef float f32x2 __attribute__((ext_vector_type(2)));
typedef float f32x4 __attribute__((ext_vector_type(4)));
typedef __bf16 bf16x2_t __attribute__((ext_vector_type(2)));
typedef __bf16 bf16x8_t __attribute__((ext_vector_type(8)));
#define GAS __attribute__((address_space(1)))
#define RLX_AGENT __ATOMIC_RELAXED, __HIP_MEMORY_SCOPE_AGENT
#define SB() __builtin_amdgcn_sched_barrier(0)
constexpr int AUX_NT = 2, AUX_SC1 = 16, RSRC_W3 = 0x00020000;

__device__ __forceinline__ __amdgpu_buffer_rsrc_t rsrc(const void* p, unsigned bytes) { return __builtin_amdgcn_make_buffer_rsrc((void*)p, 0, bytes, RSRC_W3); }
__device__ __forceinline__ f32x4 mfma16(u32x4 a, u32x4 b, f32x4 c) {
  return __builtin_amdgcn_mfma_f32_16x16x32_bf16(__builtin_bit_cast(bf16x8_t, a), __builtin_bit_cast(bf16x8_t, b), c, 0, 0, 0);
}
__device__ __forceinline__ unsigned cvtpk(float lo, float hi) {
  f32x2 v = {lo, hi};
  return __builtin_bit_cast(unsigned, __builtin_convertvector(v, bf16x2_t));
}
__device__ __forceinline__ float vmax(float a, float b) { return __builtin_elementwise_maximum(a, b); }
__device__ __forceinline__ float xrow_max(float x) {
  auto s = __builtin_amdgcn_permlane16_swap(__float_as_uint(x), __float_as_uint(x), false, false);
  x = vmax(__uint_as_float(s[0]), __uint_as_float(s[1]));
  auto t = __builtin_amdgcn_permlane32_swap(__float_as_uint(x), __float_as_uint(x), false, false);
  return vmax(__uint_as_float(t[0]), __uint_as_float(t[1]));
}
__device__ __forceinline__ float xrow16_sum(float x) {
  auto s = __builtin_amdgcn_permlane16_swap(__float_as_uint(x), __float_as_uint(x), false, false);
  return __uint_as_float(s[0]) + __uint_as_float(s[1]);
}
__device__ __forceinline__ float xrow_sum(float x) {
  x = xrow16_sum(x);
  auto t = __builtin_amdgcn_permlane32_swap(__float_as_uint(x), __float_as_uint(x), false, false);
  return __uint_as_float(t[0]) + __uint_as_float(t[1]);
}
template <int CTRL> __device__ __forceinline__ float dpp(float x) {
  return __builtin_bit_cast(float, __builtin_amdgcn_mov_dpp(__builtin_bit_cast(int, x), CTRL, 0xf, 0xf, true));
}
constexpr int ROR1 = 0x121, ROR2 = 0x122, ROR4 = 0x124, ROR8 = 0x128, QUAD_EVEN = 0xa0  , QUAD_ODD = 0xf5  ;
__device__ __forceinline__ float row_sum(float x) { x += dpp<ROR8>(x); x += dpp<ROR4>(x); x += dpp<ROR2>(x); return x + dpp<ROR1>(x); }
__device__ __forceinline__ float bflo(unsigned u) { return __uint_as_float(u << 16); }
__device__ __forceinline__ float bfhi(unsigned u) { return __uint_as_float(u & 0xffff0000u); }
__device__ __forceinline__ unsigned not_empty(float x) { return min(__float_as_uint(x), EMPTY - 1u); }
template <int N> __device__ __forceinline__ void waitvm() { asm volatile("s_waitcnt vmcnt(%0)" ::"n"(N) : "memory"); }
__device__ __forceinline__ unsigned clock16() { return (unsigned)__builtin_amdgcn_s_memrealtime() * 16u; }

__global__ void __launch_bounds__(NTHR, 2) attn_kernel(const unsigned short* __restrict__ kc, const unsigned short* __restrict__ vc, const unsigned short* __restrict__ q,
                                                       unsigned char* __restrict__ ws, const unsigned short* __restrict__ kt, const unsigned short* __restrict__ vt,
                                                       const unsigned short* __restrict__ mask, unsigned short* __restrict__ out) {
  __shared__ __attribute__((aligned(16))) float lds[L_WORDS];
  const unsigned long long tent = __builtin_amdgcn_s_memrealtime();
  unsigned long long pc0;
  asm volatile("s_getpc_b64 %0" : "=s"(pc0));
  SB();
  const int lane = threadIdx.x & 63, wave = __builtin_amdgcn_readfirstlane(threadIdx.x >> 6);
  const int grp = (blockIdx.x >> 5) * 8 + (blockIdx.x & 7), h = grp >> 2, s = (grp & 3) * GS + ((blockIdx.x >> 3) & 3);
  const unsigned wgoff = (unsigned)(grp * (GS * NSW * TILES) + (s % GS) * NSW) * 4096u;
  const int a = lane >> 4, n = lane & 15;
  unsigned base = wgoff + wave * 4096u;
  unsigned koff = (lane & 7) * 256 + (lane >> 3) * 16;
  asm volatile("" : "+s"(base));
  asm volatile("" : "+v"(koff));
  SB();
  asm volatile("" :: "s"(kc), "s"(vc), "s"(q), "s"(tent));

  if (__builtin_expect(wave == NSW, 0)) {
    unsigned char* const wsh = ws + (size_t)h * WS_HEAD;
    const __amdgpu_buffer_rsrc_t wr = rsrc(wsh, WS_HEAD), orr = rsrc(out, HQ * D * 2);
    GAS unsigned* const zw = (GAS unsigned*)(wsh + WS_Z);
    GAS unsigned* const xw = (GAS unsigned*)(wsh + WS_X);
    const bool leader = s == 4 + 8 * (h & 1) + ((h >> 1) & 3);
    float st = -INFINITY;
    f32x4 vtf = {0.f, 0.f, 0.f, 0.f};
    unsigned x = 0, xs = 0, z = 0;
#define UNTIL(t) while ((unsigned)(__builtin_amdgcn_s_memrealtime() - tent) < (t)) __builtin_amdgcn_s_sleep(16);
#define ECHO() { const unsigned xn = __hip_atomic_load(xw + n * 32, RLX_AGENT); if (lane < NSPLIT && xn != x) __hip_atomic_store(zw + lane, xn, RLX_AGENT); x = xn; }
#define TOUCH_V() { const unsigned t = *(const GAS unsigned*)((const GAS char*)vc + wgoff); asm volatile("" :: "v"(t)); }
    if (!leader) {
      xs = __builtin_amdgcn_readfirstlane(__hip_atomic_load(zw + s, RLX_AGENT)) + 1u;
      if (lane < 32) __hip_atomic_store(xw + s * 32 + lane, xs, RLX_AGENT);
      if ((blockIdx.x >> 3) == 1 || (blockIdx.x >> 3) == 2) {
        unsigned long long p = pc0 & ~0xffull;
        unsigned d;
#pragma unroll 1
        for (int i = 0; i < CODE_BYTES / 512; ++i, p += 512)
          asm volatile("s_load_dword %0, %1, 0\n\ts_load_dword %0, %1, 64\n\ts_load_dword %0, %1, 128\n\ts_load_dword %0, %1, 192\n\t"
                       "s_load_dword %0, %1, 256\n\ts_load_dword %0, %1, 320\n\ts_load_dword %0, %1, 384\n\ts_load_dword %0, %1, 448\n\ts_waitcnt lgkmcnt(0)"
                       : "=&s"(d) : "s"(p));
      }
      UNTIL(1400)
      z = __builtin_amdgcn_readfirstlane(__hip_atomic_load(zw + s, RLX_AGENT));
      TOUCH_V()
      if (z != xs) {
        UNTIL(2700)
        z = __builtin_amdgcn_readfirstlane(__hip_atomic_load(zw + s, RLX_AGENT));
      }
    } else {
      UNTIL(250)
      const u32x4 e4 = {EMPTY, EMPTY, EMPTY, EMPTY};
#pragma unroll
      for (int i = 0; i < NSPLIT; ++i) __builtin_amdgcn_raw_buffer_store_b128(e4, wr, WS_SLOT + i * SLOT_BYTES + lane * 16, 0, AUX_SC1);
#pragma unroll
      for (int i = 0; i < 2; ++i) __builtin_amdgcn_raw_buffer_store_b128(e4, wr, WS_SLOT + (8 * i + (lane >> 3)) * SLOT_BYTES + 1024 + (lane & 7) * 16, 0, AUX_SC1);
      const u32x2 qq = *(const GAS u32x2*)((const GAS char*)q + h * (G * D * 2) + lane * 8);
      const u32x2 kk = *(const GAS u32x2*)((const GAS char*)kt + h * (D * 2) + (lane & 31) * 8);
      const unsigned vv = *(const GAS unsigned*)((const GAS char*)vt + h * (D * 2) + lane * 4);
      const unsigned mk = *(const GAS unsigned short*)mask;
      float d = bflo(qq[0]) * bflo(kk[0]);
      d = __builtin_fmaf(bfhi(qq[0]), bfhi(kk[0]), d);
      d = __builtin_fmaf(bflo(qq[1]), bflo(kk[1]), d);
      d = __builtin_fmaf(bfhi(qq[1]), bfhi(kk[1]), d);
      d = xrow16_sum(row_sum(d));
      d = __shfl(d, (n >> 3) * 32);
      st = (d * SCALE + (1.0f - bflo(mk)) * -1000000000.0f) * LOG2E;
      lds[L_VT + lane] = __uint_as_float(vv);
      asm volatile("s_waitcnt lgkmcnt(0)" ::: "memory");
#pragma unroll
      for (int j = 0; j < 4; ++j) vtf[j] = bflo(((const unsigned short*)&lds[L_VT])[32 * a + 8 * j + (n & 7)]);
      waitvm<0>();
      UNTIL(900)
      x = __hip_atomic_load(xw + n * 32, RLX_AGENT);
      if (lane < 32) __hip_atomic_store(zw + lane, x, RLX_AGENT);
      TOUCH_V()
      UNTIL(2100)
      ECHO()
    }
    const bool own = (lane >> 2) == s;
    unsigned ooff = WS_SLOT + (lane >> 2) * SLOT_BYTES + s * 64 + (lane & 3) * 16, moff = WS_SLOT + (lane >> 2) * SLOT_BYTES + 1024 + ((s >> 1) & 1) * 8;
    unsigned poff = WS_SLOT + s * SLOT_BYTES + lane * 16, pmoff = (lane & 55) == 0 ? WS_SLOT + s * SLOT_BYTES + 1024 + lane : OOB;
    unsigned outoff = (lane & 12) == 0 ? (h * (G * D) + ((s >> 1) & 1) * D + 32 * (s >> 2) + 8 * a + 4 * (s & 1) + (lane & 3)) * 2 : OOB;
    float lt = a == 0 ? 1.0f : 0.0f;
    unsigned lofs = lane * 8;
    asm volatile("" : "+v"(ooff), "+v"(moff), "+v"(poff), "+v"(pmoff), "+v"(outoff), "+v"(lt), "+v"(lofs));
    __builtin_amdgcn_s_barrier();
    asm volatile("" ::: "memory");
    unsigned spins = 0;
#pragma unroll 1
    while (__builtin_expect(z != xs, 0)) {
      if (++spins > SPIN_CAP) return;
      __builtin_amdgcn_s_sleep(32);
      z = __builtin_amdgcn_readfirstlane(__hip_atomic_load(zw + s, RLX_AGENT));
    }
    f32x2 mlw[NSW];
    f32x4 ow[NSW];
#pragma unroll
    for (int w = 0; w < NSW; ++w) mlw[w] = *(const f32x2*)((const char*)&lds[L_ML + w * 128] + lofs);
#pragma unroll
    for (int w = 0; w < NSW; ++w) ow[w] = *(const f32x4*)((const char*)&lds[L_O + w * 256] + 2 * lofs);
    SB();
    float M = st;
#pragma unroll
    for (int w = 0; w < NSW; ++w) M = vmax(M, mlw[w][0]);
    const float wt0 = __builtin_amdgcn_exp2f(st - M);
    float L = wt0 * lt;
    f32x4 acc = vtf * wt0;
#pragma unroll
    for (int w = 0; w < NSW; ++w) { const float wt = __builtin_amdgcn_exp2f(mlw[w][0] - M); L = __builtin_fmaf(wt, mlw[w][1], L); acc += ow[w] * wt; }
    const u32x4 po = {not_empty(acc[0]), not_empty(acc[1]), not_empty(acc[2]), not_empty(acc[3])};
    __builtin_amdgcn_raw_buffer_store_b128(po, wr, poff, 0, AUX_SC1);
    SB();
    const u32x2 pm = {not_empty(M), not_empty(xrow_sum(L))};
    __builtin_amdgcn_raw_buffer_store_b64(pm, wr, pmoff, 0, AUX_SC1);
    SB();
    u32x4 o = po;
    u32x2 ml = pm;
    bool pend = !own;
    unsigned long long any = 1ull;
    __builtin_amdgcn_s_sleep(10);
#pragma unroll 1
    while (any != 0ull && ++spins <= SPIN_CAP) {
      if (leader && (spins & 7u) == 4u) ECHO()
      asm volatile("" ::: "memory");
      const u32x4 on = __builtin_amdgcn_raw_buffer_load_b128(wr, pend ? ooff : OOB, 0, AUX_SC1);
      const u32x2 mn = __builtin_amdgcn_raw_buffer_load_b64(wr, pend ? moff : OOB, 0, AUX_SC1);
      if (pend) { o = on; ml = mn; }
      pend = max(max(max(o[0], o[1]), max(o[2], o[3])), max(ml[0], ml[1])) == EMPTY;
      any = __builtin_amdgcn_ballot_w64(pend);
    }
    const float ms = __uint_as_float(ml[0]);
    float Mf = vmax(ms, dpp<ROR4>(ms));
    Mf = xrow_max(vmax(Mf, dpp<ROR8>(Mf)));
    const float wf = __builtin_amdgcn_exp2f(ms - Mf);
    float t[5];
    t[4] = wf * __uint_as_float(ml[1]);
#pragma unroll
    for (int k = 0; k < 4; ++k) t[k] = wf * __uint_as_float(o[k]);
    asm volatile("" : "+v"(t[0]), "+v"(t[1]), "+v"(t[2]), "+v"(t[3]), "+v"(t[4]));
#pragma unroll
    for (int k = 4; k >= 0; --k) t[k] += dpp<ROR4>(t[k]);
    asm volatile("" : "+v"(t[0]), "+v"(t[1]), "+v"(t[2]), "+v"(t[3]), "+v"(t[4]));
#pragma unroll
    for (int k = 4; k >= 0; --k) t[k] += dpp<ROR8>(t[k]);
    asm volatile("" : "+v"(t[0]), "+v"(t[1]), "+v"(t[2]), "+v"(t[3]), "+v"(t[4]));
    const float inv = __builtin_amdgcn_rcpf(xrow_sum(t[4]));
    const auto s01 = __builtin_amdgcn_permlane16_swap(__float_as_uint(t[0]), __float_as_uint(t[1]), false, false);
    const auto s23 = __builtin_amdgcn_permlane16_swap(__float_as_uint(t[2]), __float_as_uint(t[3]), false, false);
    const float u = __uint_as_float(s01[0]) + __uint_as_float(s01[1]), v = __uint_as_float(s23[0]) + __uint_as_float(s23[1]);
    const auto suv = __builtin_amdgcn_permlane32_swap(__float_as_uint(u), __float_as_uint(v), false, false);
    const float r = (__uint_as_float(suv[0]) + __uint_as_float(suv[1])) * inv;
    __builtin_amdgcn_raw_buffer_store_b16((unsigned short)cvtpk(r, r), orr, outoff, 0, 0);
    return;
  }

  const unsigned voff = a * 1024 + n * 16;
  u32x4 Q[2], R0[4], R1[4], R2[4], R3[4];
  const __amdgpu_buffer_rsrc_t kr = rsrc(kc, KV_BYTES);
#define LDK(R, o) { _Pragma("unroll") for (int j = 0; j < 4; ++j) R[j] = __builtin_amdgcn_raw_buffer_load_b128(kr, koff + (j & 1) * 128 + (j >> 1) * 2048, (o), AUX_NT); SB(); }
#define LDV(R, o, nb) { const __amdgpu_buffer_rsrc_t vr = rsrc(vc, (nb)); \
    _Pragma("unroll") for (int j = 0; j < 4; ++j) R[j] = __builtin_amdgcn_raw_buffer_load_b128(vr, voff + j * 256, (o), AUX_NT); SB(); }
  unsigned long long targs = __builtin_amdgcn_s_memrealtime();
  SB();
  LDK(R0, base)
  asm volatile("" : "+s"(targs));
  SB();
  { const __amdgpu_buffer_rsrc_t qr = rsrc(q, HQ * D * 2);
#pragma unroll
    for (int j = 0; j < 2; ++j) Q[j] = __builtin_amdgcn_raw_buffer_load_b128(qr, (n >> 3) * 256 + (2 * a + (n & 1)) * 16 + j * 128, h * (G * D * 2), 0);
    SB(); }
  LDK(R1, base + TSTEP)
  float* const sw = &lds[L_S + wave * (TILES * 32) + a * 8 + (n >> 3) * 4];
  float mx = -INFINITY;
  unsigned so = base + 2 * TSTEP;
  const unsigned cap0 = (unsigned)targs * 16u + CAP_ARGS, cap1 = clock16() + CAP_ISSUED;
  SB();
  waitvm<4>();
  asm volatile("" : "+v"(Q[0]), "+v"(Q[1]));
  SB();
  unsigned turn = clock16();
  if ((int)(turn - cap0) > 0) turn = cap0;
  if ((int)(turn - cap1) > 0) turn = cap1;
  turn += PACE - ((turn & 0x0fffffffu) + PACE - (((blockIdx.x >> 5) + blockIdx.x) & 7) * (PACE / 8)) % PACE;
#define TURN() { while ((int)(turn - clock16()) > 0) {} turn += PACE; SB(); }
#define QK(R, t) { \
    const f32x4 z4 = {0.f, 0.f, 0.f, 0.f}; \
    const f32x4 d0 = mfma16(R[1], Q[1], mfma16(R[0], Q[0], z4)), d1 = mfma16(R[3], Q[1], mfma16(R[2], Q[0], z4)); \
    f32x4 S; \
    _Pragma("unroll") for (int j = 0; j < 4; ++j) { const auto x = __builtin_amdgcn_permlane32_swap(__float_as_uint(d0[j]), __float_as_uint(d1[j]), false, false); \
      S[j] = __uint_as_float(x[0]) + dpp<QUAD_ODD>(__uint_as_float(x[1])); }   \
    mx = vmax(vmax(vmax(mx, S[0]), S[1]), vmax(S[2], S[3])); \
    if ((n & 7) == 0) *(f32x4*)&sw[(t) * 32] = S; \
    asm volatile("" : "+v"(mx)); SB(); }
#pragma unroll 1
  for (int t = 0; t < TILES - 4; t += 4) {
    TURN() LDK(R2, so) QK(R0, t)
    TURN() LDK(R3, so + TSTEP) QK(R1, t + 1)
    TURN() LDK(R0, so + 2 * TSTEP) QK(R2, t + 2)
    TURN() LDK(R1, so + 3 * TSTEP) QK(R3, t + 3)
    so += 4 * TSTEP;
  }
  TURN() LDK(R2, so) QK(R0, TILES - 4)
  TURN() LDK(R3, so + TSTEP) QK(R1, TILES - 3)
  TURN() LDV(R0, base, KV_BYTES) QK(R2, TILES - 2)
  TURN() LDV(R1, base + TSTEP, KV_BYTES) QK(R3, TILES - 1)
  const float mc = xrow_max(dpp<QUAD_EVEN>(mx)) * C2;
  unsigned msk[4];
#pragma unroll
  for (int w = 0; w < 4; ++w) msk[w] = ((n & 7) >> 1) == w ? ((n & 1) ? 0xffff0000u : 0x0000ffffu) : 0u;
  float l = 0.f;
  f32x4 O = {0.f, 0.f, 0.f, 0.f};
#define PV(R, RN, o, nb, t) { \
    const f32x4 S = *(const f32x4*)&sw[(t) * 32]; \
    float p[4]; u32x4 B[4]; \
    _Pragma("unroll") for (int j = 0; j < 4; ++j) p[j] = __builtin_amdgcn_exp2f(__builtin_fmaf(S[j], C2, -mc)); \
    l += (p[0] + p[1]) + (p[2] + p[3]); \
    _Pragma("unroll") for (int j = 0; j < 4; ++j) { const unsigned pp = cvtpk(p[j], p[j]); B[j] = u32x4{pp & msk[0], pp & msk[1], pp & msk[2], pp & msk[3]}; } \
    asm volatile("" : "+v"(B[0]), "+v"(B[1]), "+v"(B[2]), "+v"(B[3]), "+v"(l)); \
    SB(); \
    TURN() LDV(RN, o, nb) \
    _Pragma("unroll") for (int j = 0; j < 4; ++j) O = mfma16(R[j], B[j], O); \
    asm volatile("" : "+v"(O)); \
    SB(); }
  so = base + 2 * TSTEP;
#pragma unroll 1
  for (int t = 0; t < TILES; t += 4) {
    const bool more = t < TILES - 4;
    const unsigned nb = more ? KV_BYTES : 0u, so2 = more ? so : 0u;
    PV(R0, R2, so, KV_BYTES, t)
    PV(R1, R3, so + TSTEP, KV_BYTES, t + 1)
    if (!more) turn -= 1u << 24;
    PV(R2, R0, so2 + 2 * TSTEP, nb, t + 2)
    PV(R3, R1, so2 + 3 * TSTEP, nb, t + 3)
    so += 4 * TSTEP;
  }
  *(f32x2*)&lds[L_ML + wave * 128 + 2 * lane] = f32x2{mc, l};
  *(f32x4*)&lds[L_O + wave * 256 + 4 * lane] = O;
  asm volatile("s_waitcnt lgkmcnt(0)" ::: "memory");
  __builtin_amdgcn_s_barrier();
}
}

extern "C" void kernel_launch(void* const* d_in, const int* in_sizes, int n_in, void* d_out, int out_size, void* d_ws, size_t ws_size, hipStream_t stream) {
  const unsigned short* q = (const unsigned short*)d_in[0];
  const unsigned short* kc = (const unsigned short*)d_in[1];
  const unsigned short* vc = (const unsigned short*)d_in[2];
  const unsigned short* kt = (const unsigned short*)d_in[3];
  const unsigned short* vt = (const unsigned short*)d_in[4];
  const unsigned short* mask = (const unsigned short*)d_in[5];
  attn_kernel<<<HKV * NSPLIT, NTHR, 0, stream>>>(kc, vc, q, (unsigned char*)d_ws, kt, vt, mask, (unsigned short*)d_out);
}
```

```cpp
#include <hip/hip_runtime.h>
#include <hip/hip_bf16.h>
#include <stdint.h>

namespace {
constexpr int HQ = 32, HKV = 16, CTX = 32768, D = 128, G = HQ / HKV;
constexpr int NSPLIT = 16, NSW = 4;
constexpr int NTHR = 64 * (NSW + 1);
constexpr int TILES = CTX / 16 / NSPLIT / NSW;
constexpr int GS = 4;
constexpr unsigned KV_BYTES = (unsigned)HKV * CTX * D * 2;
constexpr unsigned TSTEP = GS * NSW * 4096;
constexpr float SCALE = 0.088388347648318440f, LOG2E = 1.4426950408889634f, C2 = SCALE * LOG2E;
constexpr unsigned PACE = 857, CAP_ISSUED = 90 * 16, CAP_START = 222 * 16;
constexpr unsigned WS_X = 0, WS_Z = 2048, WS_SLOT = 4096, SLOT_BYTES = 1024 + 128, WS_HEAD = 32768;
constexpr unsigned EMPTY = 0xffffffffu;
constexpr unsigned OOB = 0x40000000u;
constexpr unsigned SPIN_CAP = 1u << 20;
constexpr int CODE_BYTES = 6144;
constexpr int L_O = 0, L_ML = NSW * 256, L_VT = L_ML + NSW * 128, L_S = L_VT + 64, L_WORDS = L_S + NSW * TILES * 32;

typedef unsigned u32x2 __attribute__((ext_vector_type(2)));
typedef unsigned u32x4 __attribute__((ext_vector_type(4)));
typedef float f32x2 __attribute__((ext_vector_type(2)));
typedef float f32x4 __attribute__((ext_vector_type(4)));
typedef __bf16 bf16x2_t __attribute__((ext_vector_type(2)));
typedef __bf16 bf16x8_t __attribute__((ext_vector_type(8)));
#define GAS __attribute__((address_space(1)))
#define RLX_AGENT __ATOMIC_RELAXED, __HIP_MEMORY_SCOPE_AGENT
#define SB() __builtin_amdgcn_sched_barrier(0)
constexpr int AUX_NT = 2, AUX_SC1 = 16, RSRC_W3 = 0x00020000;

__device__ __forceinline__ __amdgpu_buffer_rsrc_t rsrc(const void* p, unsigned bytes) { return __builtin_amdgcn_make_buffer_rsrc((void*)p, 0, bytes, RSRC_W3); }
__device__ __forceinline__ f32x4 mfma16(u32x4 a, u32x4 b, f32x4 c) {
  return __builtin_amdgcn_mfma_f32_16x16x32_bf16(__builtin_bit_cast(bf16x8_t, a), __builtin_bit_cast(bf16x8_t, b), c, 0, 0, 0);
}
__device__ __forceinline__ unsigned cvtpk(float lo, float hi) {
  f32x2 v = {lo, hi};
  return __builtin_bit_cast(unsigned, __builtin_convertvector(v, bf16x2_t));
}
__device__ __forceinline__ float vmax(float a, float b) { return __builtin_elementwise_maximum(a, b); }
__device__ __forceinline__ float xrow_max(float x) {
  auto s = __builtin_amdgcn_permlane16_swap(__float_as_uint(x), __float_as_uint(x), false, false);
  x = vmax(__uint_as_float(s[0]), __uint_as_float(s[1]));
  auto t = __builtin_amdgcn_permlane32_swap(__float_as_uint(x), __float_as_uint(x), false, false);
  return vmax(__uint_as_float(t[0]), __uint_as_float(t[1]));
}
__device__ __forceinline__ float xrow16_sum(float x) {
  auto s = __builtin_amdgcn_permlane16_swap(__float_as_uint(x), __float_as_uint(x), false, false);
  return __uint_as_float(s[0]) + __uint_as_float(s[1]);
}
__device__ __forceinline__ float xrow_sum(float x) {
  x = xrow16_sum(x);
  auto t = __builtin_amdgcn_permlane32_swap(__float_as_uint(x), __float_as_uint(x), false, false);
  return __uint_as_float(t[0]) + __uint_as_float(t[1]);
}
template <int CTRL> __device__ __forceinline__ float dpp(float x) {
  return __builtin_bit_cast(float, __builtin_amdgcn_mov_dpp(__builtin_bit_cast(int, x), CTRL, 0xf, 0xf, true));
}
constexpr int ROR1 = 0x121, ROR2 = 0x122, ROR4 = 0x124, ROR8 = 0x128, QUAD_EVEN = 0xa0  , QUAD_ODD = 0xf5  ;
__device__ __forceinline__ float row_sum(float x) { x += dpp<ROR8>(x); x += dpp<ROR4>(x); x += dpp<ROR2>(x); return x + dpp<ROR1>(x); }
__device__ __forceinline__ float bflo(unsigned u) { return __uint_as_float(u << 16); }
__device__ __forceinline__ float bfhi(unsigned u) { return __uint_as_float(u & 0xffff0000u); }
__device__ __forceinline__ unsigned not_empty(float x) { return min(__float_as_uint(x), EMPTY - 1u); }
template <int N> __device__ __forceinline__ void waitvm() { asm volatile("s_waitcnt vmcnt(%0)" ::"n"(N) : "memory"); }
__device__ __forceinline__ unsigned clock16() { return (unsigned)__builtin_amdgcn_s_memrealtime() * 16u; }

__global__ void __launch_bounds__(NTHR, 2) attn_kernel(const unsigned short* __restrict__ kc, const unsigned short* __restrict__ vc, const unsigned short* __restrict__ q,
                                                       unsigned char* __restrict__ ws, const unsigned short* __restrict__ kt, const unsigned short* __restrict__ vt,
                                                       const unsigned short* __restrict__ mask, unsigned short* __restrict__ out) {
  __shared__ __attribute__((aligned(16))) float lds[L_WORDS];
  const unsigned long long tent = __builtin_amdgcn_s_memrealtime();
  unsigned long long pc0;
  asm volatile("s_getpc_b64 %0" : "=s"(pc0));
  SB();
  const int lane = threadIdx.x & 63, wave = __builtin_amdgcn_readfirstlane(threadIdx.x >> 6);
  const int grp = (blockIdx.x >> 5) * 8 + (blockIdx.x & 7), h = grp >> 2, s = (grp & 3) * GS + ((blockIdx.x >> 3) & 3);
  const unsigned wgoff = (unsigned)(grp * (GS * NSW * TILES) + (s % GS) * NSW) * 4096u;
  const int a = lane >> 4, n = lane & 15;
  unsigned base = wgoff + wave * 4096u;
  unsigned koff = (lane & 7) * 256 + (lane >> 3) * 16;
  asm volatile("" : "+s"(base));
  asm volatile("" : "+v"(koff));
  SB();
  asm volatile("" :: "s"(kc), "s"(vc), "s"(q), "s"(tent));

  if (__builtin_expect(wave == NSW, 0)) {
    unsigned char* const wsh = ws + (size_t)h * WS_HEAD;
    const __amdgpu_buffer_rsrc_t wr = rsrc(wsh, WS_HEAD), orr = rsrc(out, HQ * D * 2);
    GAS unsigned* const zw = (GAS unsigned*)(wsh + WS_Z);
    GAS unsigned* const xw = (GAS unsigned*)(wsh + WS_X);
    const bool leader = s == 4 + 8 * (h & 1) + ((h >> 1) & 3);
    float st = -INFINITY;
    f32x4 vtf = {0.f, 0.f, 0.f, 0.f};
    unsigned x = 0, xs = 0, z = 0;
#define UNTIL(t) while ((unsigned)(__builtin_amdgcn_s_memrealtime() - tent) < (t)) __builtin_amdgcn_s_sleep(16);
#define ECHO() { const unsigned xn = __hip_atomic_load(xw + n * 32, RLX_AGENT); if (lane < NSPLIT && xn != x) __hip_atomic_store(zw + lane, xn, RLX_AGENT); x = xn; }
#define TOUCH_V() { const unsigned t = *(const GAS unsigned*)((const GAS char*)vc + wgoff); asm volatile("" :: "v"(t)); }
    if (!leader) {
      xs = __builtin_amdgcn_readfirstlane(__hip_atomic_load(zw + s, RLX_AGENT)) + 1u;
      if (lane < 32) __hip_atomic_store(xw + s * 32 + lane, xs, RLX_AGENT);
      if ((blockIdx.x >> 3) == 1 || (blockIdx.x >> 3) == 2) {
        unsigned long long p = pc0 & ~0xffull;
        unsigned d;
#pragma unroll 1
        for (int i = 0; i < CODE_BYTES / 512; ++i, p += 512)
          asm volatile("s_load_dword %0, %1, 0\n\ts_load_dword %0, %1, 64\n\ts_load_dword %0, %1, 128\n\ts_load_dword %0, %1, 192\n\t"
                       "s_load_dword %0, %1, 256\n\ts_load_dword %0, %1, 320\n\ts_load_dword %0, %1, 384\n\ts_load_dword %0, %1, 448\n\ts_waitcnt lgkmcnt(0)"
                       : "=&s"(d) : "s"(p));
      }
      UNTIL(1400)
      z = __builtin_amdgcn_readfirstlane(__hip_atomic_load(zw + s, RLX_AGENT));
      TOUCH_V()
      if (z != xs) {
        UNTIL(2700)
        z = __builtin_amdgcn_readfirstlane(__hip_atomic_load(zw + s, RLX_AGENT));
      }
    } else {
      UNTIL(250)
      const u32x4 e4 = {EMPTY, EMPTY, EMPTY, EMPTY};
#pragma unroll
      for (int i = 0; i < NSPLIT; ++i) __builtin_amdgcn_raw_buffer_store_b128(e4, wr, WS_SLOT + i * SLOT_BYTES + lane * 16, 0, AUX_SC1);
#pragma unroll
      for (int i = 0; i < 2; ++i) __builtin_amdgcn_raw_buffer_store_b128(e4, wr, WS_SLOT + (8 * i + (lane >> 3)) * SLOT_BYTES + 1024 + (lane & 7) * 16, 0, AUX_SC1);
      const u32x2 qq = *(const GAS u32x2*)((const GAS char*)q + h * (G * D * 2) + lane * 8);
      const u32x2 kk = *(const GAS u32x2*)((const GAS char*)kt + h * (D * 2) + (lane & 31) * 8);
      const unsigned vv = *(const GAS unsigned*)((const GAS char*)vt + h * (D * 2) + lane * 4);
      const unsigned mk = *(const GAS unsigned short*)mask;
      float d = bflo(qq[0]) * bflo(kk[0]);
      d = __builtin_fmaf(bfhi(qq[0]), bfhi(kk[0]), d);
      d = __builtin_fmaf(bflo(qq[1]), bflo(kk[1]), d);
      d = __builtin_fmaf(bfhi(qq[1]), bfhi(kk[1]), d);
      d = xrow16_sum(row_sum(d));
      d = __shfl(d, (n >> 3) * 32);
      st = (d * SCALE + (1.0f - bflo(mk)) * -1000000000.0f) * LOG2E;
      lds[L_VT + lane] = __uint_as_float(vv);
      asm volatile("s_waitcnt lgkmcnt(0)" ::: "memory");
#pragma unroll
      for (int j = 0; j < 4; ++j) vtf[j] = bflo(((const unsigned short*)&lds[L_VT])[32 * a + 8 * j + (n & 7)]);
      waitvm<0>();
      UNTIL(900)
      x = __hip_atomic_load(xw + n * 32, RLX_AGENT);
      if (lane < 32) __hip_atomic_store(zw + lane, x, RLX_AGENT);
      TOUCH_V()
      UNTIL(2100)
      ECHO()
    }
    const bool own = (lane >> 2) == s;
    unsigned ooff = WS_SLOT + (lane >> 2) * SLOT_BYTES + s * 64 + (lane & 3) * 16, moff = WS_SLOT + (lane >> 2) * SLOT_BYTES + 1024 + ((s >> 1) & 1) * 8;
    unsigned poff = WS_SLOT + s * SLOT_BYTES + lane * 16, pmoff = (lane & 55) == 0 ? WS_SLOT + s * SLOT_BYTES + 1024 + lane : OOB;
    unsigned outoff = (lane & 12) == 0 ? (h * (G * D) + ((s >> 1) & 1) * D + 32 * (s >> 2) + 8 * a + 4 * (s & 1) + (lane & 3)) * 2 : OOB;
    float lt = a == 0 ? 1.0f : 0.0f;
    unsigned lofs = lane * 8;
    asm volatile("" : "+v"(ooff), "+v"(moff), "+v"(poff), "+v"(pmoff), "+v"(outoff), "+v"(lt), "+v"(lofs));
    __builtin_amdgcn_s_barrier();
    asm volatile("" ::: "memory");
    unsigned spins = 0;
#pragma unroll 1
    while (__builtin_expect(z != xs, 0)) {
      if (++spins > SPIN_CAP) return;
      __builtin_amdgcn_s_sleep(32);
      z = __builtin_amdgcn_readfirstlane(__hip_atomic_load(zw + s, RLX_AGENT));
    }
    f32x2 mlw[NSW];
    f32x4 ow[NSW];
#pragma unroll
    for (int w = 0; w < NSW; ++w) mlw[w] = *(const f32x2*)((const char*)&lds[L_ML + w * 128] + lofs);
#pragma unroll
    for (int w = 0; w < NSW; ++w) ow[w] = *(const f32x4*)((const char*)&lds[L_O + w * 256] + 2 * lofs);
    SB();
    float M = st;
#pragma unroll
    for (int w = 0; w < NSW; ++w) M = vmax(M, mlw[w][0]);
    const float wt0 = __builtin_amdgcn_exp2f(st - M);
    float L = wt0 * lt;
    f32x4 acc = vtf * wt0;
#pragma unroll
    for (int w = 0; w < NSW; ++w) { const float wt = __builtin_amdgcn_exp2f(mlw[w][0] - M); L = __builtin_fmaf(wt, mlw[w][1], L); acc += ow[w] * wt; }
    const u32x4 po = {not_empty(acc[0]), not_empty(acc[1]), not_empty(acc[2]), not_empty(acc[3])};
    __builtin_amdgcn_raw_buffer_store_b128(po, wr, poff, 0, AUX_SC1);
    SB();
    const u32x2 pm = {not_empty(M), not_empty(xrow_sum(L))};
    __builtin_amdgcn_raw_buffer_store_b64(pm, wr, pmoff, 0, AUX_SC1);
    SB();
    u32x4 o = po;
    u32x2 ml = pm;
    bool pend = !own;
    unsigned long long any = 1ull;
    __builtin_amdgcn_s_sleep(10);
#pragma unroll 1
    while (any != 0ull && ++spins <= SPIN_CAP) {
      if (leader && (spins & 7u) == 4u) ECHO()
      asm volatile("" ::: "memory");
      const u32x4 on = __builtin_amdgcn_raw_buffer_load_b128(wr, pend ? ooff : OOB, 0, AUX_SC1);
      const u32x2 mn = __builtin_amdgcn_raw_buffer_load_b64(wr, pend ? moff : OOB, 0, AUX_SC1);
      if (pend) { o = on; ml = mn; }
      pend = max(max(max(o[0], o[1]), max(o[2], o[3])), max(ml[0], ml[1])) == EMPTY;
      any = __builtin_amdgcn_ballot_w64(pend);
    }
    const float ms = __uint_as_float(ml[0]);
    float Mf = vmax(ms, dpp<ROR4>(ms));
    Mf = xrow_max(vmax(Mf, dpp<ROR8>(Mf)));
    const float wf = __builtin_amdgcn_exp2f(ms - Mf);
    float t[5];
    t[4] = wf * __uint_as_float(ml[1]);
#pragma unroll
    for (int k = 0; k < 4; ++k) t[k] = wf * __uint_as_float(o[k]);
    asm volatile("" : "+v"(t[0]), "+v"(t[1]), "+v"(t[2]), "+v"(t[3]), "+v"(t[4]));
#pragma unroll
    for (int k = 4; k >= 0; --k) t[k] += dpp<ROR4>(t[k]);
    asm volatile("" : "+v"(t[0]), "+v"(t[1]), "+v"(t[2]), "+v"(t[3]), "+v"(t[4]));
#pragma unroll
    for (int k = 4; k >= 0; --k) t[k] += dpp<ROR8>(t[k]);
    asm volatile("" : "+v"(t[0]), "+v"(t[1]), "+v"(t[2]), "+v"(t[3]), "+v"(t[4]));
    const float inv = __builtin_amdgcn_rcpf(xrow_sum(t[4]));
    const auto s01 = __builtin_amdgcn_permlane16_swap(__float_as_uint(t[0]), __float_as_uint(t[1]), false, false);
    const auto s23 = __builtin_amdgcn_permlane16_swap(__float_as_uint(t[2]), __float_as_uint(t[3]), false, false);
    const float u = __uint_as_float(s01[0]) + __uint_as_float(s01[1]), v = __uint_as_float(s23[0]) + __uint_as_float(s23[1]);
    const auto suv = __builtin_amdgcn_permlane32_swap(__float_as_uint(u), __float_as_uint(v), false, false);
    const float r = (__uint_as_float(suv[0]) + __uint_as_float(suv[1])) * inv;
    __builtin_amdgcn_raw_buffer_store_b16((unsigned short)cvtpk(r, r), orr, outoff, 0, 0);
    return;
  }

  const unsigned voff = a * 1024 + n * 16;
  u32x4 Q[2], R0[4], R1[4], R2[4], R3[4];
  const __amdgpu_buffer_rsrc_t kr = rsrc(kc, KV_BYTES);
#define LDK(R, o) { _Pragma("unroll") for (int j = 0; j < 4; ++j) R[j] = __builtin_amdgcn_raw_buffer_load_b128(kr, koff + (j & 1) * 128 + (j >> 1) * 2048, (o), AUX_NT); SB(); }
#define LDV(R, o, nb) { const __amdgpu_buffer_rsrc_t vr = rsrc(vc, (nb)); \
    _Pragma("unroll") for (int j = 0; j < 4; ++j) R[j] = __builtin_amdgcn_raw_buffer_load_b128(vr, voff + j * 256, (o), AUX_NT); SB(); }
  LDK(R0, base)
  { const __amdgpu_buffer_rsrc_t qr = rsrc(q, HQ * D * 2);
#pragma unroll
    for (int j = 0; j < 2; ++j) Q[j] = __builtin_amdgcn_raw_buffer_load_b128(qr, (n >> 3) * 256 + (2 * a + (n & 1)) * 16 + j * 128, h * (G * D * 2), 0);
    SB(); }
  LDK(R1, base + TSTEP)
  float* const sw = &lds[L_S + wave * (TILES * 32) + a * 8 + (n >> 3) * 4];
  float mx = -INFINITY;
  unsigned so = base + 2 * TSTEP;
  const unsigned cap0 = (unsigned)tent * 16u + CAP_START, cap1 = clock16() + CAP_ISSUED;
  SB();
  waitvm<4>();
  asm volatile("" : "+v"(Q[0]), "+v"(Q[1]));
  SB();
  unsigned turn = clock16();
  if ((int)(turn - cap0) > 0) turn = cap0;
  if ((int)(turn - cap1) > 0) turn = cap1;
  turn += PACE - ((turn & 0x0fffffffu) + PACE - (((blockIdx.x >> 5) + blockIdx.x) & 7) * (PACE / 8)) % PACE;
#define TURN() { while ((int)(turn - clock16()) > 0) {} turn += PACE; SB(); }
#define QK(R, t) { \
    const f32x4 z4 = {0.f, 0.f, 0.f, 0.f}; \
    const f32x4 d0 = mfma16(R[1], Q[1], mfma16(R[0], Q[0], z4)), d1 = mfma16(R[3], Q[1], mfma16(R[2], Q[0], z4)); \
    f32x4 S; \
    _Pragma("unroll") for (int j = 0; j < 4; ++j) { const auto x = __builtin_amdgcn_permlane32_swap(__float_as_uint(d0[j]), __float_as_uint(d1[j]), false, false); \
      S[j] = __uint_as_float(x[0]) + dpp<QUAD_ODD>(__uint_as_float(x[1])); }   \
    mx = vmax(vmax(vmax(mx, S[0]), S[1]), vmax(S[2], S[3])); \
    if ((n & 7) == 0) *(f32x4*)&sw[(t) * 32] = S; \
    asm volatile("" : "+v"(mx)); SB(); }
#pragma unroll 1
  for (int t = 0; t < TILES - 4; t += 4) {
    TURN() LDK(R2, so) QK(R0, t)
    TURN() LDK(R3, so + TSTEP) QK(R1, t + 1)
    TURN() LDK(R0, so + 2 * TSTEP) QK(R2, t + 2)
    TURN() LDK(R1, so + 3 * TSTEP) QK(R3, t + 3)
    so += 4 * TSTEP;
  }
  TURN() LDK(R2, so) QK(R0, TILES - 4)
  TURN() LDK(R3, so + TSTEP) QK(R1, TILES - 3)
  TURN() LDV(R0, base, KV_BYTES) QK(R2, TILES - 2)
  TURN() LDV(R1, base + TSTEP, KV_BYTES) QK(R3, TILES - 1)
  asm volatile("" ::: "memory");
  const float mc = xrow_max(dpp<QUAD_EVEN>(mx)) * C2;
  unsigned msk[4];
#pragma unroll
  for (int w = 0; w < 4; ++w) msk[w] = ((n & 7) >> 1) == w ? ((n & 1) ? 0xffff0000u : 0x0000ffffu) : 0u;
  float l = 0.f;
  f32x4 O = {0.f, 0.f, 0.f, 0.f};
#define PV(R, RN, o, nb, t) { \
    const f32x4 S = *(const f32x4*)&sw[(t) * 32]; \
    float p[4]; u32x4 B[4]; \
    _Pragma("unroll") for (int j = 0; j < 4; ++j) p[j] = __builtin_amdgcn_exp2f(__builtin_fmaf(S[j], C2, -mc)); \
    l += (p[0] + p[1]) + (p[2] + p[3]); \
    _Pragma("unroll") for (int j = 0; j < 4; ++j) { const unsigned pp = cvtpk(p[j], p[j]); B[j] = u32x4{pp & msk[0], pp & msk[1], pp & msk[2], pp & msk[3]}; } \
    asm volatile("" : "+v"(B[0]), "+v"(B[1]), "+v"(B[2]), "+v"(B[3]), "+v"(l)); \
    SB(); \
    TURN() LDV(RN, o, nb) \
    _Pragma("unroll") for (int j = 0; j < 4; ++j) O = mfma16(R[j], B[j], O); \
    asm volatile("" : "+v"(O)); \
    SB(); }
  so = base + 2 * TSTEP;
#pragma unroll 1
  for (int t = 0; t < TILES; t += 4) {
    const bool more = t < TILES - 4;
    const unsigned nb = more ? KV_BYTES : 0u, so2 = more ? so : 0u;
    PV(R0, R2, so, KV_BYTES, t)
    PV(R1, R3, so + TSTEP, KV_BYTES, t + 1)
    if (!more) turn -= 1u << 24;
    PV(R2, R0, so2 + 2 * TSTEP, nb, t + 2)
    PV(R3, R1, so2 + 3 * TSTEP, nb, t + 3)
    so += 4 * TSTEP;
  }
  *(f32x2*)&lds[L_ML + wave * 128 + 2 * lane] = f32x2{mc, l};
  *(f32x4*)&lds[L_O + wave * 256 + 4 * lane] = O;
  asm volatile("s_waitcnt lgkmcnt(0)" ::: "memory");
  __builtin_amdgcn_s_barrier();
}
}

extern "C" void kernel_launch(void* const* d_in, const int* in_sizes, int n_in, void* d_out, int out_size, void* d_ws, size_t ws_size, hipStream_t stream) {
  const unsigned short* q = (const unsigned short*)d_in[0];
  const unsigned short* kc = (const unsigned short*)d_in[1];
  const unsigned short* vc = (const unsigned short*)d_in[2];
  const unsigned short* kt = (const unsigned short*)d_in[3];
  const unsigned short* vt = (const unsigned short*)d_in[4];
  const unsigned short* mask = (const unsigned short*)d_in[5];
  attn_kernel<<<HKV * NSPLIT, NTHR, 0, stream>>>(kc, vc, q, (unsigned char*)d_ws, kt, vt, mask, (unsigned short*)d_out);
}
```
